```python
import torch

def _softmax(x, axis=-1):
    return torch.softmax(x, dim=axis)

def _gelu(x):
    return torch.nn.functional.gelu(x, approximate='tanh')

def _silu(x):
    return torch.nn.functional.silu(x)

def _sigmoid(x):
    return torch.sigmoid(x)

def _layernorm(x, axis=-1, eps=1e-05):
    m = x.mean(dim=axis, keepdim=True)
    v = x.var(dim=axis, keepdim=True, unbiased=False)
    return (x - m) / torch.sqrt(v + eps)

def _scatter_add(tgt, idx, src, axis=0):
    return tgt.clone().index_add_(axis, idx, src)

def _segment_sum(src, seg, n):
    out = torch.zeros((n,) + src.shape[1:], dtype=src.dtype)
    return out.index_add_(0, seg, src)

def _pad(x, pw):
    flat = tuple((v for pair in reversed(pw) for v in pair))
    return torch.nn.functional.pad(x, flat)
kernel_name = 'adaLN_cond__sliding_local_dilated__moe_arch_mod__prompt'

def setup_inputs():

    _w = lambda a: (lambda t: t.to(torch.bfloat16) if t.is_floating_point() else t)(torch.as_tensor(a))

    def _A_setup():
        B, S, D = (1, 2048, 640)
        return {'x': _w((lambda *s: torch.randn(*s, dtype=torch.bfloat16) * 0.05)(B, S, D)), 'wqkv': _w((lambda *s: torch.randn(*s, dtype=torch.bfloat16) * 0.05)(D, 3 * D)), 'wo': _w((lambda *s: torch.randn(*s, dtype=torch.bfloat16) * 0.05)(D, D))}
    _A_in = _A_setup()

    def _B_setup():
        B, S, D, F, C = (1, 2048, 640, 1728, 1)
        return {'x': _w((lambda *s: torch.randn(*s, dtype=torch.bfloat16) * 0.05)(B, S, D)), 'w_r': _w((lambda *s: torch.randn(*s, dtype=torch.bfloat16) * 3.2)(D)), 'w_gate': _w((lambda *s: torch.randn(*s, dtype=torch.bfloat16) * 0.1)(D, F)), 'w_up': _w((lambda *s: torch.randn(*s, dtype=torch.bfloat16) * 0.1)(D, F)), 'w_down': _w((lambda *s: torch.randn(*s, dtype=torch.bfloat16) * 0.1)(F, D))}
    _B_in = _B_setup()
    return {'A_x': _A_in['x'], 'A_wqkv': _A_in['wqkv'], 'A_wo': _A_in['wo'], 'B_x': _B_in['x'], 'B_w_r': _B_in['w_r'], 'B_w_gate': _B_in['w_gate'], 'B_w_up': _B_in['w_up'], 'B_w_down': _B_in['w_down'], 'c': _w((lambda *s: torch.randn(*s, dtype=torch.bfloat16) * 0.1)(1, 640) * 8.0), 'w_mod': _w((lambda *s: torch.randn(*s, dtype=torch.bfloat16) * 0.1)(640, 6 * 640) * 640 ** (-0.5) * 8.0)}

def reference(A_x, A_wqkv, A_wo, B_x, B_w_r, B_w_gate, B_w_up, B_w_down, c, w_mod):

    def _A_ref(x, wqkv, wo):
        B, S, D = x.shape
        H = 32
        Dh = D // H
        W, dil = (8, 2)
        qkv = (lambda x, *d: x.permute(*d))((x @ wqkv).reshape(B, S, 3, H, Dh), 2, 0, 3, 1, 4)
        q, k, v = (qkv[0], qkv[1], qkv[2])
        scale = Dh ** (-0.5)
        pos = torch.arange(S)
        off = torch.arange(W) * dil
        idx = pos[:, None] - off[None, :]
        mask = idx >= 0
        idx_c = torch.where(mask, idx, 0).reshape(-1)
        kg = k[:, :, idx_c, :].reshape(B, H, S, W, Dh)
        vg = v[:, :, idx_c, :].reshape(B, H, S, W, Dh)
        logits = torch.einsum('bhsd,bhswd->bhsw', q, kg) * scale
        logits = torch.where(mask[None, None, :, :], logits, -1000000000.0)
        attn = _softmax(logits, axis=-1)
        o = torch.einsum('bhsw,bhswd->bhsd', attn, vg)
        return (lambda x, *d: x.permute(*d))(o, 0, 2, 1, 3).reshape(B, S, D) @ wo

    def _B_ref(x, w_r, w_gate, w_up, w_down):
        B, S, D = x.shape
        C = 1
        r = x @ w_r
        idx = torch.argsort(-r, axis=-1)[:, :C]
        idx_e = (lambda x, n, axis: torch.repeat_interleave(x, n, dim=axis))(idx.reshape(B, C, 1), D, axis=2)
        x_sel = (lambda x, i, axis: torch.take_along_dim(x, i, dim=axis))(x, idx_e, axis=1)
        g = _silu(x_sel @ w_gate)
        u = x_sel @ w_up
        y = g * u @ w_down
        r_sel = _sigmoid((lambda x, i, axis: torch.take_along_dim(x, i, dim=axis))(r, idx, axis=1))
        y = y * r_sel.reshape(B, C, 1)
        b_off = (torch.arange(B) * S).reshape(B, 1)
        flat_idx = (idx + b_off).reshape(B * C)
        zero = (x * 0.0).reshape(B * S, D)
        upd = _scatter_add(zero, flat_idx, y.reshape(B * C, D), axis=0)
        return x + upd.reshape(B, S, D)
    x = A_x
    mod = (_silu(c) @ w_mod).reshape(c.shape[0], 6, 1, -1)
    sa, ca, ga, sb, cb, gb = (mod[:, 0].clone(), mod[:, 1].clone(), mod[:, 2].clone(), mod[:, 3].clone(), mod[:, 4].clone(), mod[:, 5].clone())
    h = _layernorm(x, axis=-1) * (1 + ca) + sa
    x = x + ga * _A_ref(h, A_wqkv, A_wo)
    h = _layernorm(x, axis=-1) * (1 + cb) + sb
    x = x + gb * _B_ref(h, B_w_r, B_w_gate, B_w_up, B_w_down)
    return x
```

```cpp
#include <hip/hip_runtime.h>
#include <stdint.h>

typedef __attribute__((ext_vector_type(8))) short bf16x8;
typedef __attribute__((ext_vector_type(4))) short s16x4;
typedef __attribute__((ext_vector_type(4))) float f32x4;
typedef __attribute__((ext_vector_type(2))) float f32x2;
typedef __attribute__((ext_vector_type(4))) unsigned u32x4;
typedef __attribute__((ext_vector_type(2))) unsigned u32x2;
typedef __attribute__((ext_vector_type(2))) __bf16 bf16x2;
typedef unsigned short u16;
typedef unsigned long long u64;
#define LAS __attribute__((address_space(3)))

#define GSL 2
#define WO1 4
#define TW 2
#define KS 8
#define S_LEN 2048
#define DM 640
#define FF 1728
#define SC1 16
#define YD 16
#define KN 30
#define YS 2
#define XS 5
#define PGI 9
#define O1 8
#define SPIN_CAP (1 << 18)
#define SENT 0xffffffffu
#define OOB 0x40000000

__device__ __forceinline__ float bf2f(unsigned v) { return __uint_as_float(v << 16); }
__device__ __forceinline__ unsigned pack2v(f32x2 v) {
  bf16x2 r = __builtin_convertvector(v, bf16x2);
  return __builtin_bit_cast(unsigned, r);
}
__device__ __forceinline__ unsigned pack2(float a, float b) { return pack2v((f32x2){a, b}); }
__device__ __forceinline__ float rbf(float f) { return __uint_as_float(pack2(f, 0.f) << 16); }
__device__ __forceinline__ f32x2 unpk(unsigned u) {
  f32x2 r;
  r.x = __uint_as_float(u << 16);
  r.y = __uint_as_float(u & 0xffff0000u);
  return r;
}
__device__ __forceinline__ f32x2 rbf2(f32x2 v) { return unpk(pack2v(v)); }
__device__ __forceinline__ unsigned nosent(unsigned u) { return u < 0xfffffffeu ? u : 0xfffffffeu; }
__device__ __forceinline__ unsigned umax(unsigned a, unsigned b) { return a > b ? a : b; }
__device__ __forceinline__ unsigned max4(u32x4 v, unsigned m) { return umax(umax(umax(v.x, v.y), v.z), umax(v.w, m)); }
__device__ __forceinline__ float dot2(unsigned a, unsigned b, float c) {
  return __builtin_amdgcn_fdot2_f32_bf16(__builtin_bit_cast(bf16x2, a), __builtin_bit_cast(bf16x2, b), c, false);
}

template <int CTRL> __device__ __forceinline__ float dppf(float v) {
  return __builtin_bit_cast(float, __builtin_amdgcn_update_dpp(0, __builtin_bit_cast(int, v), CTRL, 0xF, 0xF, true));
}
template <int CTRL> __device__ __forceinline__ unsigned dppu(unsigned v) {
  return (unsigned)__builtin_amdgcn_update_dpp(0, (int)v, CTRL, 0xF, 0xF, true);
}
__device__ __forceinline__ float swapsum16(float v) {
  auto r = __builtin_amdgcn_permlane16_swap(__float_as_uint(v), __float_as_uint(v), false, false);
  return __uint_as_float(r[0]) + __uint_as_float(r[1]);
}
__device__ __forceinline__ float swapsum32(float v) {
  auto r = __builtin_amdgcn_permlane32_swap(__float_as_uint(v), __float_as_uint(v), false, false);
  return __uint_as_float(r[0]) + __uint_as_float(r[1]);
}
__device__ __forceinline__ float sum64(float v) {
  v += dppf<0xB1>(v); v += dppf<0x4E>(v); v += dppf<0x141>(v); v += dppf<0x140>(v);
  return swapsum32(swapsum16(v));
}
__device__ __forceinline__ unsigned max64(unsigned v) {
  v = umax(v, dppu<0xB1>(v)); v = umax(v, dppu<0x4E>(v)); v = umax(v, dppu<0x141>(v)); v = umax(v, dppu<0x140>(v));
  auto r = __builtin_amdgcn_permlane16_swap(v, v, false, false);
  v = umax(r[0], r[1]);
  auto q = __builtin_amdgcn_permlane32_swap(v, v, false, false);
  return umax(q[0], q[1]);
}
typedef __attribute__((ext_vector_type(4))) short v4i16_t;
__device__ __forceinline__ s16x4 trread(const char* p) {
  return __builtin_bit_cast(s16x4, __builtin_amdgcn_ds_read_tr16_b64_v4i16((LAS v4i16_t*)p));
}
__device__ __forceinline__ u32x4 ld128(__amdgpu_buffer_rsrc_t rs, int off) {
  asm volatile("" : "+v"(off));
  return __builtin_amdgcn_raw_buffer_load_b128(rs, off, 0, SC1);
}
__device__ __forceinline__ u32x2 ld64(__amdgpu_buffer_rsrc_t rs, int off) {
  asm volatile("" : "+v"(off));
  return __builtin_amdgcn_raw_buffer_load_b64(rs, off, 0, SC1);
}
__device__ __forceinline__ unsigned ld32(__amdgpu_buffer_rsrc_t rs, int off) {
  asm volatile("" : "+v"(off));
  return __builtin_amdgcn_raw_buffer_load_b32(rs, off, 0, SC1);
}
#define ST128(v, off) __builtin_amdgcn_raw_buffer_store_b128((v), rs, (off), 0, SC1)
#define ST64(v, off) __builtin_amdgcn_raw_buffer_store_b64((v), rs, (off), 0, SC1)
#define ST32(v, off) __builtin_amdgcn_raw_buffer_store_b32((v), rs, (off), 0, SC1)
#define SBAR() __builtin_amdgcn_sched_barrier(0)
#define CFENCE() asm volatile("" ::: "memory")
#define LBAR() do { asm volatile("s_waitcnt lgkmcnt(0)" ::: "memory"); __builtin_amdgcn_s_barrier(); CFENCE(); } while (0)

#define STAMP_INIT() do {} while (0)
#define STAMP_DUMP() do {} while (0)
#define STAMP(i) do {} while (0)

#define LD128S(voff, soff) __builtin_amdgcn_raw_buffer_load_b128(rs, (voff), (soff), SC1)
#define DRAIN() asm volatile("s_waitcnt vmcnt(0)" ::: "memory")

#define WS_EPOCH 0
#define WS_FLAGP 4096
#define WS_FLAGW 8192
#define WS_FLAGO 20480
#define WS_STAT 40960
#define WS_MODF 69632
#define WS_MODP 131072
#define WS_CB (WS_MODP + 20 * 30720)
#define WS_WT (WS_CB + 8 * 16 * 2048)
#define WS_WOT (WS_WT + 16 * 20 * 8192)
#define WS_O (WS_WOT + 8 * 100 * 1024)
#define WS_HALO (WS_O + S_LEN * DM * 2)
#define WS_ST2 (WS_HALO + 256 * 5120)
#define WS_BEST (WS_ST2 + 2 * 128 * 4096)
#define WS_KEY (WS_BEST + 128 * 5120)
#define WS_YP (WS_KEY + 8 * 128 * KS)
#define WS_END (WS_YP + 108 * 5120)

#define L_MISC 0
#define L_CNT 256
#define L_MRED 512
#define L_DUM 2560
#define L_STG 4096
#define L_CS 4096
#define L_X9 14336
#define L_X 16384
#define L_LT(j) ((j) < 2 ? 6144 + (j) * 4096 : 45056 + (j) * 4096)
#define L_S 5120
#define KVS 248
#define L_KV 16384
#define L_GU 10240
#define L_WO 53248
#define L_H2 L_S
#define L_PAR 7680
#define C_XA 0
#define C_XB 1
#define C_XC 2
#define C_XO 3
#define C_HL 16
#define C_CH 17
#define C_PR 18
#define C_GO 8
#define C_OI 10
#define C_SI 11
#define C_GL 12
#define C_SL 13
#define C_KP 15
#define C_AD 24
#define C_OF 25
#define C_WL 26
#define C_YD 14
#define C_HD 20
#define C_KV 22
#define C_X9 4
#define C_FP 7
#define C_UI 6
__device__ __forceinline__ unsigned opaque(unsigned v) { asm("" : "+s"(v)); return v; }
#define ONE_LO opaque(0x00003f80u)
#define ONE_HI opaque(0x3f800000u)

__device__ __forceinline__ bool tag_ok(u32x4 g, unsigned tagl, unsigned tagh) {
  return ((((g.x ^ tagl) | (g.z ^ tagl)) & 0xffff0000u) | (g.y ^ tagh) | (g.w ^ tagh)) == 0u;
}
__device__ __forceinline__ float rs32(float a, float b) {
  auto r = __builtin_amdgcn_permlane32_swap(__float_as_uint(a), __float_as_uint(b), false, false);
  return __uint_as_float(r[0]) + __uint_as_float(r[1]);
}
__device__ __forceinline__ float rs16(float a, float b) {
  auto r = __builtin_amdgcn_permlane16_swap(__float_as_uint(a), __float_as_uint(b), false, false);
  return __uint_as_float(r[0]) + __uint_as_float(r[1]);
}
__device__ __forceinline__ float vmax(float a, float b) {
  float r;
  asm("v_max_f32 %0, %1, %2" : "=v"(r) : "v"(a), "v"(b));
  return r;
}
template <bool MAX> __device__ __forceinline__ void rows2(float& a, float& b) {
  auto r = __builtin_amdgcn_permlane16_swap(__float_as_uint(a), __float_as_uint(b), false, false);
  float t = MAX ? vmax(__uint_as_float(r[0]), __uint_as_float(r[1])) : __uint_as_float(r[0]) + __uint_as_float(r[1]);
  auto q = __builtin_amdgcn_permlane32_swap(__float_as_uint(t), __float_as_uint(t), false, false);
  t = MAX ? vmax(__uint_as_float(q[0]), __uint_as_float(q[1])) : __uint_as_float(q[0]) + __uint_as_float(q[1]);
  auto u = __builtin_amdgcn_permlane16_swap(__float_as_uint(t), __float_as_uint(t), false, false);
  a = __uint_as_float(u[0]); b = __uint_as_float(u[1]);
}
__device__ __forceinline__ unsigned padd(unsigned p, float al, float ah) { return pack2(dot2(p, ONE_LO, al), dot2(p, ONE_HI, ah)); }
__device__ __forceinline__ unsigned pmul(unsigned p, f32x2 m) { return pack2v(unpk(p) * m); }
__device__ __forceinline__ void wait_flags(__amdgpu_buffer_rsrc_t rs, int off, unsigned tagl, unsigned tagh) {
  int sp = 0;
  for (;;) {
    const u32x4 f = ld128(rs, off);
    if (__all(tag_ok(f, tagl, tagh)) || ++sp > SPIN_CAP) break;
    __builtin_amdgcn_s_sleep(2);
  }
  CFENCE();
}
__device__ __forceinline__ void wait_all256(__amdgpu_buffer_rsrc_t rs, int off, unsigned tagl, unsigned tagh) {
  int sp = 0;
  for (;;) {
    const u32x4 f0 = ld128(rs, off), f1 = ld128(rs, off + 1024);
    if (__all(tag_ok(f0, tagl, tagh) & tag_ok(f1, tagl, tagh)) || ++sp > SPIN_CAP) break;
    __builtin_amdgcn_s_sleep(8);
  }
  CFENCE();
}
__device__ __forceinline__ unsigned silu2(unsigned c) {
  const f32x2 f = unpk(c);
  return pack2(f.x * __builtin_amdgcn_rcpf(1.0f + __expf(-f.x)), f.y * __builtin_amdgcn_rcpf(1.0f + __expf(-f.y)));
}
template <int I> struct IC { static constexpr int v = I; };
template <int A, int B, class F> __device__ __forceinline__ void sfor(F&& f) {
  if constexpr (A + 1 == B) f(IC<A>{});
  else if constexpr (A < B) { sfor<A, (A + B) / 2>(f); sfor<(A + B) / 2, B>(f); }
}
template <int N> __device__ __forceinline__ void vmwait() { asm volatile("s_waitcnt vmcnt(%0)" ::"n"(N) : "memory"); }
#define MFMA(c, a, b) asm volatile("v_mfma_f32_16x16x32_bf16 %0, %1, %2, %0" : "+v"(c) : "v"(a), "v"(b))
#define MFMA0(c, a, b) asm volatile("v_mfma_f32_16x16x32_bf16 %0, %1, %2, 0" : "=&v"(c) : "v"(a), "v"(b))
#define LDX(d, voff, soff) asm volatile("buffer_load_dwordx4 %0, %1, %2, %3 offen sc1" : "=&v"(d) : "v"(voff), "s"(rs), "s"(soff) : "memory")
#define LDXO(d, voff, soff, o) asm volatile("buffer_load_dwordx4 %0, %1, %2, %3 offen offset:%4 sc1" : "=&v"(d) : "v"(voff), "s"(rs), "s"(soff), "n"(o) : "memory")
#define READY(d) asm volatile("" : "+v"(d))
#define LDXI(d, r, voff, soff) asm volatile("buffer_load_dwordx4 %0, %1, %2, %3 offen" : "=&v"(d) : "v"(voff), "s"(r), "s"(soff) : "memory")
#define LDXW(d, r, voff, soff) asm volatile("buffer_load_dword %0, %1, %2, %3 offen" : "=&v"(d) : "v"(voff), "s"(r), "s"(soff) : "memory")
#define LDXH(d, r, voff, soff) asm volatile("buffer_load_ushort %0, %1, %2, %3 offen" : "=&v"(d) : "v"(voff), "s"(r), "s"(soff) : "memory")
template <int LOFF, int OFF> __device__ __forceinline__ void dmai(__amdgpu_buffer_rsrc_t r, int voff, int soff, unsigned lds0) {
  asm volatile("s_add_u32 m0, %3, %4\n\ts_nop 0\n\tbuffer_load_dwordx4 %0, %1, %2 offen offset:%5 lds"
               :: "v"(voff), "s"(r), "s"(soff), "s"(lds0), "n"(LOFF - OFF), "n"(OFF) : "m0", "scc", "memory");
}
template <int LOFF, int OFF> __device__ __forceinline__ void dmas(__amdgpu_buffer_rsrc_t r, int voff, int soff, unsigned lds0) {
  asm volatile("s_add_u32 m0, %3, %4\n\ts_nop 0\n\tbuffer_load_dwordx4 %0, %1, %2 offen offset:%5 sc1 lds"
               :: "v"(voff), "s"(r), "s"(soff), "s"(lds0), "n"(LOFF - OFF), "n"(OFF) : "m0", "scc", "memory");
}
__device__ __forceinline__ void wait_flag8(__amdgpu_buffer_rsrc_t rs, int off, u32x2 want) {
  unsigned n;
  u32x2 f;
  asm volatile("s_mov_b32 %1, %5\n"
               "1:\n\tbuffer_load_dwordx2 %0, %2, %3, 0 offen sc1\n\ts_waitcnt vmcnt(0)\n\tv_cmp_ne_u64 vcc, %4, %0\n\ts_cbranch_vccz 2f\n\t"
               "s_sleep 2\n\ts_sub_u32 %1, %1, 1\n\ts_cbranch_scc0 1b\n2:"
               : "=&v"(f), "=&s"(n) : "v"(off), "s"(rs), "s"(want), "n"(SPIN_CAP) : "vcc", "scc", "memory");
}
#define CODE_BYTES 44800
#define NS1 3
#define GT 150
#define WT 0
#define XDL 8
#define WRD 4
#define U0 8
#define UR 3
#define SP 12
#define XT 6
#define XW 8
#define LM 2
#define HOLD 0
#define HP 0
#define FM 0
#define WDL 18
#define WO2 8
#define UD 1
#define NOLATE 0
#define NOFILL 0
#define NOTOUCH 0
#define HLE 8
#define HP2 0
#define WO3 0
#define FLP 2
#define FAI 7
#define PLX (slot & 1)
#define PTX 2
#define TW2 6
#define WA 0
#define WB 12
#define LP 0
#define XL 12
#define XG 0
#define TS 0
#define TI 3
#define NA 25
#define NB1 0
#define BD 0
#define LG 1
#define CBG 1
#define NOWO 0
#define HALFW 0
#define XT1 3
#define LPR 0
#define WP 0
#define NOWOL 0
#define NOXH 0
#define QW 16
#define OG 2
#define ODL 8
#define SDR 1
#define SDL 8
#define G1N 0
#define G2N 6
#define KG 0
#define RW 3
#define PDL 0
#define PW 7
#define SRS 2
#define GAUX 16
#define SAUX 16
#define TK 6
#define T2G C_OI
#define X1L 0
#define XP 2
#define HSG 136
constexpr int xslot(int pair) { return pair == 0 || pair == 9 ? 3 : pair < 4 ? pair - 1 : pair; }
constexpr int uissued(int s) { const int n = U0 + UR * (s + 1) / UD; return n > 40 ? 40 : n; }
constexpr int ulast() { int s = 0; while (uissued(s) < 40) ++s; return s; }
constexpr int gm(int g) { return g < 8 * SP ? g & 7 : ((g - 8 * SP) / (20 - SP) + 7) & 7; }
constexpr int gs(int g) { return g < 8 * SP ? g >> 3 : SP + (g - 8 * SP) % (20 - SP); }
template <int J> __device__ __forceinline__ float mulb(float a, float b) {
  float r = dppf<0x150 + J>(a) * b;
  asm("" : "+v"(r));
  return r;
}
__device__ __forceinline__ float fma1(float a, float b, float c) {
  float r = __builtin_fmaf(a, b, c);
  asm("" : "+v"(r));
  return r;
}
__device__ __forceinline__ void until(unsigned t) {
  while ((int)((unsigned)wall_clock64() - t) < 0) __builtin_amdgcn_s_sleep(1);
}
__device__ __forceinline__ bool none(unsigned v) {
  asm("" : "+v"(v));
  return __builtin_amdgcn_ballot_w64(v != 0u) == 0ull;
}
__device__ __forceinline__ unsigned off2(u32x4 g, unsigned tag) {
  unsigned r = (g.y ^ tag) | (g.w ^ tag);
  asm("" : "+v"(r));
  return r;
}
__device__ __forceinline__ float add1(float a, float b) {
  float r = a + b;
  asm("" : "+v"(r));
  return r;
}
__device__ __forceinline__ u32x4 keys(__amdgpu_buffer_rsrc_t rs, int rep, int l) {
  if (KS == 8) return ld128(rs, WS_KEY + rep * 1024 + l * 16);
  const u32x2 a = ld64(rs, WS_KEY + (rep * 128 + l) * KS), c = ld64(rs, WS_KEY + (rep * 128 + 64 + l) * KS);
  return (u32x4){a.x, a.y, c.x, c.y};
}
__device__ __forceinline__ int opq(int v) { asm volatile("" : "+s"(v)); return v; }
__device__ __forceinline__ void ready(u32x4& d) { READY(d); }
__device__ __forceinline__ void ldxi(u32x4& d, __amdgpu_buffer_rsrc_t r, int voff, int soff) { LDXI(d, r, voff, soff); }
#define ALL4 0x01010101u
__device__ __forceinline__ void lds_set(char* lds, int idx, int byte) { *((volatile LAS unsigned char*)(lds + L_CNT) + idx * 4 + byte) = 1; }
__device__ __forceinline__ unsigned lds_cnt(char* lds, int idx) { return *((volatile LAS unsigned*)(lds + L_CNT) + idx); }
__device__ __forceinline__ void lds_wait(char* lds, int idx, unsigned m) {
  unsigned v, c, n;
  asm volatile("s_mov_b32 %2, %5\n"
               "1:\n\tds_read_b32 %0, %3\n\ts_waitcnt lgkmcnt(0)\n\tv_readfirstlane_b32 %1, %0\n\ts_andn2_b32 %1, %4, %1\n\ts_cbranch_scc0 2f\n\t"
               "s_sleep 1\n\ts_sub_u32 %2, %2, 1\n\ts_cbranch_scc0 1b\n2:"
               : "=&v"(v), "=&s"(c), "=&s"(n) : "v"((unsigned)(size_t)(LAS char*)lds + L_CNT + idx * 4), "s"(m), "n"(SPIN_CAP) : "scc", "memory");
}
__device__ __forceinline__ float sum8(float v) {
  v += dppf<0xB1>(v); v += dppf<0x4E>(v);
  return v + dppf<0x141>(v);
}
__device__ __forceinline__ unsigned normalize(unsigned p, float mb, float isd) {
  return pack2v(rbf2(unpk(p) - (f32x2){mb, mb}) * (f32x2){isd, isd});
}

__global__ __launch_bounds__(512, 2) void k_all(const u16*, const u16*, const u16*, const u16*, const u16*, const u16*,
                                                const u16*, const u16*, const u16*, char*, u16*) {
  __shared__ __attribute__((aligned(256))) char lds[163840];
  typedef __attribute__((ext_vector_type(8))) u64 u64x8;
  typedef __attribute__((ext_vector_type(2))) u64 u64x2;
  u64x8 ka0;
  u64x2 ka1;
  u64 ka2;
  asm volatile("s_load_dwordx16 %0, %3, 0\n\ts_load_dwordx4 %1, %3, 0x40\n\ts_load_dwordx2 %2, %3, 0x50"
               : "=&s"(ka0), "=&s"(ka1), "=&s"(ka2) : "s"(__builtin_amdgcn_kernarg_segment_ptr()));
  int t = threadIdx.x, l = t & 63;
  const int b = blockIdx.x, xcd = b & 7, slot = b >> 3;
  unsigned pfc;
  {
    const __amdgpu_buffer_rsrc_t rcode = __builtin_amdgcn_make_buffer_rsrc((void*)__builtin_amdgcn_s_getpc(), 0, CODE_BYTES, 0x00020000);
    LDXW(pfc, rcode, t * 4096, slot * 128);
  }
  const int w = __builtin_amdgcn_readfirstlane(t >> 6);
  int g4 = l >> 4, i16 = l & 15;
  const int mi = w ^ 4;
  const int mk = (slot & 1) * 4 + (xcd & 3), ml = (slot >> 1) + 10 - (xcd >> 2) * 10;
  const bool hasm = w == 3 && slot < 20;
  const bool prod = slot < 30;
  const int rb = (xcd >> 1) * 4 + (slot >> 3), m0 = slot & 7, hp = (xcd & 1) * 8 + m0;
  const int rg = ((xcd + 4) & 7) * 4 + (slot >> 3), cg = slot & 7;
  const int rc = rb * 16 + m0 * 2 + (xcd & 1);
  const unsigned lbase = (unsigned)(size_t)(LAS char*)lds;
  const int o0 = 2 * w + min(w, 2);
  const int Fp = (255 - b) * 4 + w, kc = Fp / 10, ln = Fp - kc * 10, hq5 = kc / 5;
  int xo = (l >> 3) * 1280 + (((l & 7) ^ (l >> 3)) * 16);
  int mo = (l >> 3) * 15360 + (l & 7) * 16, co2 = l * 4, wsrc0 = (l >> 3) * 3840 + (l & 7) * 16;
  int osrc0 = (((l >> 3) & 3) + 20 * (l >> 5)) * 1280 + (l & 7) * 16;
  int mso = mk * 80 * 7680 + ml * 128, cso = mk * 160;
  *(unsigned*)(lds + L_CNT + l * 4) = 0u;
  asm volatile("" : "+v"(xo), "+v"(mo), "+v"(co2), "+v"(wsrc0), "+v"(osrc0));
  asm volatile("" : "+s"(mso), "+s"(cso));
  STAMP_INIT();
  SBAR();
  asm volatile("s_waitcnt lgkmcnt(0)" : "+s"(ka0), "+s"(ka1), "+s"(ka2));
  const u16 *x = (const u16*)ka0[0], *wqkv = (const u16*)ka0[1], *wo = (const u16*)ka0[2], *wr = (const u16*)ka0[3];
  const u16 *wg = (const u16*)ka0[4], *wu = (const u16*)ka0[5], *wd = (const u16*)ka0[6], *cvec = (const u16*)ka0[7];
  const u16* wmod = (const u16*)ka1[0];
  char* ws = (char*)ka1[1];
  u16* out = (u16*)ka2;
  const __amdgpu_buffer_rsrc_t rs = __builtin_amdgcn_make_buffer_rsrc(ws, 0, WS_END, 0x00020000);
  const __amdgpu_buffer_rsrc_t rx = __builtin_amdgcn_make_buffer_rsrc((void*)x, 0, S_LEN * DM * 2, 0x00020000);

  const int lu = Fp - 4, ll = lu / 20, lk = lu - ll * 20;
  u32x4 wm[10], rp, cl;
  unsigned cv;
  u32x2 epv;
  const __amdgpu_buffer_rsrc_t rm = __builtin_amdgcn_make_buffer_rsrc((void*)wmod, 0, DM * 7680, 0x00020000);
  const __amdgpu_buffer_rsrc_t rcv = __builtin_amdgcn_make_buffer_rsrc((void*)cvec, 0, DM * 2, 0x00020000);
  if (hasm) {
    LDXW(cv, rcv, co2, cso);
#pragma unroll
    for (int i = 0; i < 10; ++i) LDXI(wm[i], rm, mo, mso + (16 * (i >> 1) + (i & 1)) * 7680);
  }
  asm volatile("buffer_load_dwordx2 %0, off, %1, 0 sc1" : "=&v"(epv) : "s"(rs) : "memory");
  u64 t0;
  asm volatile("s_memrealtime %0" : "=s"(t0));
  SBAR();
  __builtin_amdgcn_s_barrier();
  SBAR();
  if (opq(w) < 4) {
    if (WRD) __builtin_amdgcn_s_sleep(WRD);
    const int s1 = (xcd * 80 + o0 * 8) * 3840 + slot * 128, s2 = (hq5 * 40 + (kc - hq5 * 5) * 4) * 1280 + ln * 128;
    const unsigned d1 = lbase + o0 * 1024;
    const __amdgpu_buffer_rsrc_t rw = __builtin_amdgcn_make_buffer_rsrc((void*)wqkv, 0, prod ? DM * 3840 : 0, 0x00020000);
    dmai<L_STG, 0>(rw, wsrc0, s1, d1);
    dmai<L_STG + 1024, 0>(rw, wsrc0, s1 + 8 * 3840, d1);
    if (w < 2) dmai<L_STG + 2048, 0>(rw, wsrc0, s1 + 16 * 3840, d1);
    const __amdgpu_buffer_rsrc_t r1 = __builtin_amdgcn_make_buffer_rsrc((void*)wo, 0, Fp < 800 ? DM * 1280 : 0, 0x00020000);
    LDXI(rp, r1, osrc0, s2);
  } else {
    LDXI(cl, rcv, g4 * 16, lu < 800 ? lk * 64 : 0);
    if (XDL) __builtin_amdgcn_s_sleep(XDL);
    const int s1 = (rb * 128 + (2 * m0 + (w & 1)) * 8) * 1280;
    const unsigned d1 = lbase + (2 * m0 + (w & 1)) * 1024;
    if (w < 6) {
      sfor<0, 5>([&](auto I) { constexpr int pr = decltype(I)::v; dmai<L_X + xslot(pr) * 16384, pr * 128>(rx, xo, s1, d1); });
    } else {
      sfor<5, 9>([&](auto I) { constexpr int pr = decltype(I)::v; dmai<L_X + xslot(pr) * 16384, pr * 128>(rx, xo, s1, d1); });
      dmai<L_X9, 9 * 128>(rx, xo, s1, lbase + (w & 1) * 1024);
    }
  }
  asm volatile("s_waitcnt lgkmcnt(0)" : "+s"(t0));
  STAMP(30);
  unsigned tagl, tagh, tag32, ep0, ep1;
  u32x2 flagv;
#define MAKE_TAG(n) do { \
    asm volatile("s_waitcnt vmcnt(%4)\n\tv_readfirstlane_b32 %0, %2\n\tv_readfirstlane_b32 %1, %3" : "=s"(ep0), "=s"(ep1) : "v"(epv.x), "v"(epv.y), "n"(n) : "memory"); \
    const u64 tag = ((((u64)ep1 << 32) | ep0) + 1ull) & 0xffffffffffffull; \
    tagl = (unsigned)(tag << 16); tagh = (unsigned)(tag >> 16); tag32 = (unsigned)tag; \
    flagv = (u32x2){tagl | 1u, tagh}; \
    asm volatile("" : "+s"(tagl), "+s"(tagh), "+s"(tag32), "+s"(flagv)); \
  } while (0)
  if (hasm) {
    int bpa = (l >> 3) * 4;
    int mdst = (l & 8) ? OOB : WS_MODP + mk * 30720 + (ml * 64 + 8 * (l & 7) + 4 * (l >> 5) + 2 * ((l >> 4) & 1)) * 8;
    float acc[8];
#pragma unroll
    for (int jj = 0; jj < 8; ++jj) acc[jj] = 0.f;
    asm volatile("" : "+v"(bpa), "+v"(mdst), "+v"(acc[0]), "+v"(acc[1]), "+v"(acc[2]), "+v"(acc[3]), "+v"(acc[4]), "+v"(acc[5]), "+v"(acc[6]), "+v"(acc[7]));
    vmwait<14>();
    READY(cv);
    const unsigned sp = silu2(cv);
    unsigned sv[5];
#pragma unroll
    for (int i = 0; i < 5; ++i) sv[i] = __builtin_amdgcn_ds_bpermute(bpa + 32 * i, sp);
    SBAR();
    sfor<0, 5>([&](auto I) {
      constexpr int i = decltype(I)::v;
      vmwait<12 - 2 * i>();
      ready(wm[2 * i]); ready(wm[2 * i + 1]);
#pragma unroll
      for (int jj = 0; jj < 4; ++jj) {
        acc[2 * jj] = dot2(__builtin_amdgcn_perm(wm[2 * i + 1][jj], wm[2 * i][jj], 0x05040100u), sv[i], acc[2 * jj]);
        acc[2 * jj + 1] = dot2(__builtin_amdgcn_perm(wm[2 * i + 1][jj], wm[2 * i][jj], 0x07060302u), sv[i], acc[2 * jj + 1]);
      }
      SBAR();
    });
    float v0 = rs32(acc[0], acc[4]), v1 = rs32(acc[1], acc[5]), v2 = rs32(acc[2], acc[6]), v3 = rs32(acc[3], acc[7]);
    v0 = rs16(v0, v2); v1 = rs16(v1, v3);
    v0 += dppf<0x128>(v0); v1 += dppf<0x128>(v1);
    MAKE_TAG(3);
    ST128(((u32x4){__float_as_uint(v0), tag32, __float_as_uint(v1), tag32}), mdst);
  }
  STAMP(3);
  const int Ggu = (slot >> 2) * 8 + xcd;
  const bool is_gu = slot < 16 && Ggu < 27;
  const int fb = Ggu * 4 + (slot & 3);
  const bool is_dn = b >= 128 && b < 168;
  const int dnb = b - 128;
  const int m2 = w & 3, kh = w >> 2;
  f32x4 mbias;
  int odst, odst4, kbo, vbo, qa[2][4];
  unsigned me;
#define ADDR_BLOCK() do { \
    const int lo = (rb == 0 && mi == 0) ? 8 : 0; \
    _Pragma("unroll") \
    for (int jj = 0; jj < 4; ++jj) { \
      const int kk = 4 * g4 + jj; \
      mbias[jj] = (kk >= lo && kk >= (i16 >> 1) + 1 && kk <= (i16 >> 1) + 8) ? 0.f : -1.0e30f; \
    } \
    const int mt = rb * 8 + mi, kc1 = 5 * hp + g4, kc4 = 5 * hp + 4; \
    odst = WS_O + (mt * 20 + (kc1 >> 2)) * 1024 + ((kc1 & 3) * 16 + i16) * 16; \
    odst4 = l < 16 ? WS_O + (mt * 20 + (kc4 >> 2)) * 1024 + ((kc4 & 3) * 16 + i16) * 16 : OOB; \
    kbo = L_KV + (16 * mi + 2 * i16) * KVS + g4 * 8; \
    vbo = L_KV + (16 * mi + 8 * g4 + 2 * ((l >> 2) & 3)) * KVS + 80 + (l & 3) * 8; \
      \
    const int qo = L_KV + (16 + 16 * mi + i16) * KVS + 160 + g4 * 8, zo = L_CNT + 128 + i16 * 8; \
    _Pragma("unroll") \
    for (int par = 0; par < 2; ++par) { \
      const bool pm = (i16 & 1) == par; \
      qa[par][0] = pm ? qo : zo; \
      qa[par][1] = (pm && g4 == 0) ? qo + 32 : zo; \
      qa[par][2] = (pm && g4 != 0) ? qo + 32 : zo; \
      qa[par][3] = (pm && g4 < 2) ? qo + 64 : zo; \
      asm volatile("" : "+v"(qa[par][0]), "+v"(qa[par][1]), "+v"(qa[par][2]), "+v"(qa[par][3])); \
    } \
    me = (i16 & 1) ? 0u : 0xffffffffu; \
    asm volatile("" : "+v"(odst), "+v"(odst4), "+v"(mbias), "+v"(kbo), "+v"(vbo), "+v"(me)); \
  } while (0)

  u32x4 wov[NA];
  u32x4 hv[5];
  int hlo;
  if (opq(w) < 4) {
    int rdst, cdst, goff, fh, fpo, odsto, tra = L_STG + o0 * 1024 + (i16 >> 2) * 128 + g4 * 32 + (i16 & 3) * 8;
    int twa = L_STG + o0 * 1024 + l * 16, mra = L_MRED + l * 8;
    int tro = ln < 8 ? L_STG + o0 * 1024 + (i16 >> 2) * 128 + (g4 >> 1) * 64 + (i16 & 3) * 16 + (g4 & 1) * 8 : tra;
    {
      const int co = slot * 8 + (l >> 3), part = co >= 160 ? 2 : co >= 80 ? 1 : 0, r = co - part * 80, hh = (r * 205) >> 10;
      const int pos = (part == 0 ? 80 : part * 40 - 40) + (r - hh * 5) * 8 + (l & 7);
      rdst = (prod && (!HALFW || (l & 8))) ? hh * 163840 + (pos >> 4) * 1024 + (pos & 15) * 16 : OOB;
      cdst = prod ? WS_CB + (xcd * 16 + hh) * 2048 + pos * 16 : OOB;
      const int np = w < 2 ? 11 : 7;
      goff = WS_MODP + g4 * 30720 + (xcd * 80 + o0 * 8 + 2 * (i16 < np ? i16 : np)) * 8;
      fh = l == 0 ? (prod ? WS_FLAGW + b * 16 : OOB) : (l == 1 ? WS_FLAGP + b * 8 : OOB);
      const int cls = (w == 0 ? 1 : w == 3 ? 0 : (l & 16) ? 2 : (w == 1 ? 1 : 0));
      fpo = WS_FLAGW + ((cls * 80 + 5 * hp + ((l >> 3) & 1) * 4) >> 3) * 128 + (l & 7) * 16;
      const int fo = ln < 8 ? ln * 102400 + g4 * 1024 : (4 * (ln - 8) + g4) * 102400 + 4096;
      odsto = (Fp < 800 && !NOWO) ? WS_WOT + fo + (kc >> 2) * 5120 + (kc & 3) * 256 + i16 * 16 : OOB;
    }
    unsigned cnt = l == 0 ? 1u : 0u, ca = lbase + L_CNT + ((C_HD + l) & 63) * 4;
    asm volatile("" : "+v"(rdst), "+v"(cdst), "+v"(goff), "+v"(fh), "+v"(fpo), "+v"(odsto), "+v"(tra), "+v"(tro), "+v"(twa), "+v"(mra), "+v"(cnt), "+v"(ca));
    int wso[3];
#pragma unroll
    for (int r = 0; r < 3; ++r) { const int ko = xcd * 10 + o0 + r; wso[r] = opq(WS_WT + (ko >> 2) * 8192 + (ko & 3) * 256); }
    STAMP(2);
    ADDR_BLOCK();
    const int wsrc = WS_WT + hp * 163840 + w * 2048;
    int uo0 = l * 16, uo1 = (w < 3 || i16 < 8) ? l * 16 + 1024 : OOB;
    int xb0 = L_X + (i16 >> 3) * 1024 + (i16 & 7) * 128 + ((g4 ^ (i16 & 7)) * 16), xb1 = xb0 ^ 64;
    int xc0 = xb0 + 65536, xc1 = xb1 + 65536, xd0 = xb0 + 131072, xd1 = xb1 + 131072;
    int kvo0 = L_KV + (16 + i16) * KVS + w * 64 + g4 * 8;
    int kvo1 = kvo0 + 32 - ((w == 3 && g4 == 3) ? 8 : 0);
    int hdo0 = w < 3 ? WS_HALO + (rb * 16 + hp) * 5120 + w * 2048 + l * 16 : OOB, hdo1 = w < 2 ? hdo0 + 1024 : OOB;
    int cso = L_CS + w * 128 + g4 * 16, sto = L_CS + 1024 + i16 * 8;
    asm volatile("" : "+v"(uo0), "+v"(uo1), "+v"(xb0), "+v"(xb1), "+v"(xc0), "+v"(xc1), "+v"(xd0), "+v"(xd1));
    asm volatile("" : "+v"(kvo0), "+v"(kvo1), "+v"(hdo0), "+v"(hdo1), "+v"(cso), "+v"(sto));
    STAMP(31);
    until((unsigned)t0 + GT);
    {
      u32x4 g[2], h[2];
      LDX(g[0], goff, 5120); LDX(g[1], goff, 122880 + 5120);
      LDX(h[0], goff, 0); LDX(h[1], goff, 122880);
    if (hasm) vmwait<4>(); else MAKE_TAG(4);
    asm volatile("" ::"v"(pfc));
    READY(rp);
    STAMP(4);
    f32x2 wf[3][4];
    u32x4 wraw[3];
#pragma unroll
    for (int r = 0; r < 3; ++r) {
      const u32x2 lo = __builtin_bit_cast(u32x2, trread(lds + tra + r * 1024)), hi = __builtin_bit_cast(u32x2, trread(lds + tra + r * 1024 + 512));
      wraw[r] = (u32x4){lo.x, lo.y, hi.x, hi.y};
    }
    CFENCE();
    *(u32x4*)(lds + twa) = rp;
    CFENCE();
    {
      const u32x2 lo = __builtin_bit_cast(u32x2, trread(lds + tro)), hi = __builtin_bit_cast(u32x2, trread(lds + tro + 512));
      ST128(((u32x4){lo.x, lo.y, hi.x, hi.y}), odsto);
    }
#pragma unroll
    for (int r = 0; r < 3; ++r)
#pragma unroll
      for (int jj = 0; jj < 4; ++jj) { wf[r][jj] = unpk(wraw[r][jj]); asm volatile("" : "+v"(wf[r][jj])); }
      STAMP(5);
      vmwait<3>();
      READY(g[0]); READY(g[1]);
#define GOOD2(g) none(off2(g[0], tag32) | off2(g[1], tag32))
#define RETRY2(g, so) do { \
        if (__builtin_expect(!GOOD2(g), 0)) { \
          int sp_ = 0; \
          do { \
            __builtin_amdgcn_s_sleep(2); \
            g[0] = ld128(rs, goff + (so)); g[1] = ld128(rs, goff + 122880 + (so)); \
          } while (!GOOD2(g) && ++sp_ < SPIN_CAP); \
        } \
      } while (0)
#define GOOD4(g) none(off2(g[0], tag32) | off2(g[1], tag32) | off2(g[2], tag32) | off2(g[3], tag32))
#define RETRY4(g, vo, st, so) do { \
        if (__builtin_expect(!GOOD4(g), 0)) { \
          int sp_ = 0; \
          do { \
            __builtin_amdgcn_s_sleep(2); \
            _Pragma("unroll") for (int q = 0; q < 4; ++q) g[q] = ld128(rs, (vo) + q * (st) + (so)); \
          } while (!GOOD4(g) && ++sp_ < SPIN_CAP); \
        } \
      } while (0)
#define SUM4(g, c) add1(add1(__uint_as_float(g[0].c), __uint_as_float(g[1].c)), add1(__uint_as_float(g[2].c), __uint_as_float(g[3].c)))
      RETRY2(g, 5120);
      STAMP(6);
      float a = __uint_as_float(g[0].x) + __uint_as_float(g[1].x), c = __uint_as_float(g[0].z) + __uint_as_float(g[1].z);
      rows2<false>(a, c);
      const f32x2 c1 = rbf2(rbf2((f32x2){a, c}) + (f32x2){1.0f, 1.0f});
      u32x4 wp[3];
      sfor<0, 3>([&](auto R) {
        constexpr int r = decltype(R)::v;
        if (r < 2 || w < 2) {
          sfor<0, 4>([&](auto J) {
            constexpr int jj = decltype(J)::v;
            wp[r][jj] = pack2(mulb<4 * r + jj>(c1.x, wf[r][jj].x), mulb<4 * r + jj>(c1.y, wf[r][jj].y));
          });
          __builtin_amdgcn_raw_buffer_store_b128(wp[r], rs, rdst, wso[r], SC1);
        }
      });
      SBAR();
      STAMP(1);
      if (w < 2) vmwait<4>(); else vmwait<3>();
      READY(h[0]); READY(h[1]);
      RETRY2(h, 0);
      a = __uint_as_float(h[0].x) + __uint_as_float(h[1].x); c = __uint_as_float(h[0].z) + __uint_as_float(h[1].z);
      rows2<false>(a, c);
      const unsigned sa2 = pack2(a, c);
      float cs = 0.f, bi = 0.f;
      sfor<0, 3>([&](auto R) {
        constexpr int r = decltype(R)::v;
        if (r < 2 || w < 2) {
          sfor<0, 4>([&](auto J) {
            constexpr int jj = decltype(J)::v;
            cs = dot2(wp[r][jj], 0x3f803f80u, cs);
            bi = dot2(wraw[r][jj], dppu<0x150 + 4 * r + jj>(sa2), bi);
          });
        }
      });
      *(f32x2*)(lds + mra + w * 512) = (f32x2){cs, bi};
    }
    asm volatile("s_waitcnt vmcnt(0)\n\tds_add_rtn_u32 %0, %1, %0\n\ts_waitcnt lgkmcnt(0)" : "+v"(cnt) : "v"(ca) : "memory");
    if (__builtin_amdgcn_readfirstlane(cnt) == 3u) {
      ST64(flagv, fh);
      const f32x2 v = (*(const f32x2*)(lds + mra) + *(const f32x2*)(lds + mra + 512)) + (*(const f32x2*)(lds + mra + 1024) + *(const f32x2*)(lds + mra + 1536));
      ST128(((u32x4){__float_as_uint(v.x), tag32, __float_as_uint(v.y), tag32}), cdst);
    }
    STAMP(28);

    if (PW & 1) ST64(((u32x2){0u, ~tag32}), WS_ST2 + ((rg * 4 + w) * 8 + cg) * 512 + l * 8);
    if (PW & 2) ST64(((u32x2){0u, ~tag32}), l == 0 ? WS_KEY + (cg * 128 + rg * 4 + w) * KS : OOB);
    STAMP(29);
    if (WDL) { lds_wait(lds, C_HD, 4u); __builtin_amdgcn_s_sleep(WDL); }
    wait_flag8(rs, fpo, flagv);
    STAMP(16);
    u32x4 u[40];
    f32x4 acc[8][2];
    auto uld = [&](auto K) __attribute__((always_inline)) {
      constexpr int k = decltype(K)::v;
      u[k] = LD128S((k & 1) ? uo1 : uo0, wsrc + (k >> 1) * 8192);
    };
    sfor<0, U0>(uld);
    SBAR();
    lds_set(lds, C_UI, w);
    lds_wait(lds, C_XA, ALL4);
    constexpr int NB = 20 - SP;
    static_assert(SP >= 10 && NB >= 8, "");
    auto xfrag = [&](int g) __attribute__((always_inline)) {
      const int m = gm(g), s = gs(g), sl = xslot(s >> 1);
      const int base = sl < 4 ? ((s & 1) ? xb1 : xb0) : sl < 8 ? ((s & 1) ? xc1 : xc0) : ((s & 1) ? xd1 : xd0);
      return *(const bf16x8*)(lds + base + (sl & 3) * 16384 + m * 2048);
    };
    constexpr int XR = XW + 2;
    static_assert(XW % 2 == 0, "");
    bf16x8 xq[XR];
#pragma unroll
    for (int g = 0; g < XW; ++g) { xq[g ^ 1] = xfrag(g ^ 1); SBAR(); }
    SBAR();
    f32x4 cs[2], bi[2], et[2];
    f32x2 st[2];
    u32x2 pk;
    unsigned rdy, rdys;
    auto epi = [&](auto M, auto K) __attribute__((always_inline)) {
      constexpr int m = decltype(M)::v, n = decltype(K)::v / 11, k = decltype(K)::v % 11;
      if constexpr (n < 2) {
        if constexpr (k < 4) et[n][k] = fma1(cs[n][k], st[m & 1].y, bi[n][k]);
        else if constexpr (k < 8) et[n][k - 4] = fma1(acc[m][n][k - 4], st[m & 1].x, et[n][k - 4]);
        else if constexpr (k == 8) pk.x = pack2v(et[n].xy);
        else if constexpr (k == 9) pk.y = pack2v(et[n].zw);
        else {
          if constexpr (m == 7) __builtin_amdgcn_raw_buffer_store_b128(((u32x4){pk.x, tag32, pk.y, tag32}), rs, n ? hdo1 : hdo0, 0, SC1);
          *(u32x2*)(lds + (n ? kvo1 : kvo0) + m * 16 * KVS) = pk;
        }
      }
    };
    constexpr int G1 = 48 - XW, G2 = 80 - XW, G3 = 8 * SP + NB + 3, G4 = 8 * SP + NB - 2 - XW;
    static_assert(G4 >= G2 + 5 && G3 >= G4 + 5, "");
    sfor<0, 160>([&](auto G) {
      constexpr int g = decltype(G)::v, m = gm(g), s = gs(g), e = g < 8 * SP ? -1 : (g - 8 * SP) % NB;
      constexpr bool ep = g >= 8 * SP + NB;
      constexpr int pm = ep ? gm(g - NB) : 0;
      if constexpr (g == G1 || g == G2 || g == G3 || g == G4) {
        if (__builtin_expect(rdys != ALL4, 0)) lds_wait(lds, g == G1 ? C_XB : g == G2 ? C_XC : g == G3 ? C_KV : C_X9, ALL4);
        CFENCE();
      }
      if (s == 0) MFMA0(acc[m][0], __builtin_bit_cast(bf16x8, u[2 * s]), xq[g % XR]);
      else MFMA(acc[m][0], __builtin_bit_cast(bf16x8, u[2 * s]), xq[g % XR]);
      SBAR();
      if constexpr (g + XW < 160) xq[((g + XW) ^ 1) % XR] = xfrag((g + XW) ^ 1);
      if constexpr (ep) epi(IC<pm>{}, IC<3 * e>{});
      SBAR();
      if (s == 0) MFMA0(acc[m][1], __builtin_bit_cast(bf16x8, u[2 * s + 1]), xq[g % XR]);
      else MFMA(acc[m][1], __builtin_bit_cast(bf16x8, u[2 * s + 1]), xq[g % XR]);
      SBAR();
      {
        constexpr int sg = g >> 3, n0 = uissued(sg - 1), n1 = uissued(sg), f = g & 7;
        if constexpr ((f & 1) == 0 && n0 + f / 2 < n1) uld(IC<n0 + f / 2>{});
      }
      if constexpr (ep) { epi(IC<pm>{}, IC<3 * e + 1>{}); epi(IC<pm>{}, IC<3 * e + 2>{}); }
      if constexpr (g + 5 == G1 || g + 5 == G2 || g + 5 == G3 || g + 5 == G4) rdy = lds_cnt(lds, g + 5 == G1 ? C_XB : g + 5 == G2 ? C_XC : g + 5 == G3 ? C_KV : C_X9);
      if constexpr (g + 2 == G1 || g + 2 == G2 || g + 2 == G3 || g + 2 == G4) rdys = __builtin_amdgcn_readfirstlane(rdy);
      if constexpr (g >= 8 * SP + 1 && g < 8 * SP + 9 && (g & 1)) {
        constexpr int i = (g - 8 * SP) / 2;
        if constexpr (i < 2) cs[i] = *(const f32x4*)(lds + cso + i * 64); else bi[i - 2] = *(const f32x4*)(lds + cso + 512 + (i - 2) * 64);
      }
      if constexpr (e == NB - 5) st[m & 1] = *(const f32x2*)(lds + sto + m * 128);
      if constexpr (g == 16) lds_set(lds, C_CH, w);
      if constexpr (g == 64) lds_set(lds, C_KV, w);
      if constexpr (g == 8 * ulast() + 7 - HLE) lds_set(lds, C_HL, w);
      if constexpr (g == HSG) lds_set(lds, C_GO, w);
      SBAR();
    });
    sfor<0, 4>([&](auto K) { epi(IC<6>{}, K); epi(IC<6>{}, IC<11 + decltype(K)::v>{}); });
    SBAR();
    asm volatile("s_nop 1" ::: "memory");
    SBAR();
    STAMP(22);
    sfor<4, 11>([&](auto K) { epi(IC<6>{}, K); });
    sfor<15, 22>([&](auto K) { epi(IC<6>{}, K); });
  } else {
    const int j = w - 4;
    int sb[4];
    unsigned ld[4];
#pragma unroll
    for (int i = 0; i < 4; ++i) {
      const int oc = (2 * m0 + 4 * j + i) & 15;
      sb[i] = opq((NOXH && (i & 1)) ? OOB : (rb * 128 + oc * 8) * 1280); ld[i] = opq(lbase + oc * 1024);
    }
    ADDR_BLOCK();
    const int gq = j * 8 + xcd;
    const char* pfa;
    {
      const int pt = l < 20 ? 0 : l < 40 ? 1 : 2, pr = slot * 20 + min(l - pt * 20, 19);
      pfa = (const char*)(pt == 0 ? wg : pt == 1 ? wu : wd) + (gq < 27 ? (pt < 2 ? pr * FF * 2 + gq * 128 : (gq * 640 + pr) * 128) : 0);
      if (TK < 10) {
        const int r = slot * 2 * TK + min(l & 31, 2 * TK - 1);
        pfa = (const char*)(l < 32 ? wg : wu) + (gq < 27 ? (r < 32 * TK ? r : r + 320 - 32 * TK) * FF * 2 + gq * 128 : 0);
      }
      asm volatile("" : "+v"(pfa));
    }
    hlo = rb > 0 ? WS_HALO + ((rb - 1) * 16 + hp) * 5120 + l * 16 : OOB;
    const int cc = 32 * j + (l & 31);
    int cbo = WS_CB + ((l >> 5) * 64 + hp) * 2048 + (cc < 120 ? cc : 119) * 16, sgo = WS_STAT + (rb * 128 + cc) * 8;
    int cws = L_CS + (l >> 5) * 512 + cc * 4, sws = L_CS + 1024 + cc * 8, l16 = l * 16;
    asm volatile("" : "+v"(cbo), "+v"(sgo), "+v"(cws), "+v"(sws), "+v"(l16), "+v"(hlo));
    const int wob0 = opq(NOWOL ? OOB : WS_WOT + cg * 102400 + j * 25600);
    int x9a[4];
#pragma unroll
    for (int i = 0; i < 4; ++i) { x9a[i] = L_X + xslot(9) * 16384 + ((2 * m0 + 4 * j + i) & 15) * 1024 + l16; asm volatile("" : "+v"(x9a[i])); }
    int lta = L_LT(j) + g4 * 1024 + (i16 >> 2) * 128 + (i16 & 3) * 8;
    int lsrc = (l >> 3) * 7680 + (l & 7) * 16;
    int ldst = (lu < 800 && !(i16 & 2)) ? WS_MODP + lk * 30720 + ((20 + ll) * 64 + (i16 >> 2) * 16 + g4 * 4 + (i16 & 1) * 2) * 8 : OOB;
    int fpo = WS_FLAGP + (64 * j + l) * 8;
    asm volatile("" : "+v"(lta), "+v"(lsrc), "+v"(ldst), "+v"(fpo));
    const __amdgpu_buffer_rsrc_t rl = __builtin_amdgcn_make_buffer_rsrc((void*)wmod, 0, (lu < 800 && !NOLATE) ? DM * 7680 : 0, 0x00020000);
    const int lso = opq(lk * 32 * 7680 + (20 + ll) * 128);
    MAKE_TAG(5);
    asm volatile("" ::"v"(pfc));
#define TOUCH() do { if (!NOTOUCH) asm volatile("s_mov_b32 m0, %1\n\ts_nop 0\n\tglobal_load_lds_dword %0, off" :: "v"(pfa), "s"(lbase + L_DUM) : "m0", "memory"); } while (0)
    u32x4 lb[4];
    {
      READY(cl);
      const u32x4 sc = {silu2(cl.x), silu2(cl.y), silu2(cl.z), silu2(cl.w)};
#pragma unroll
      for (int i = 0; i < 4; ++i) { lb[i] = (i16 >> 2) == i ? sc : (u32x4){0u, 0u, 0u, 0u}; asm volatile("" : "+v"(lb[i])); }
    }
    STAMP(2);
    auto stream = [&](auto F, auto A, auto B) __attribute__((always_inline)) {
      constexpr int f = decltype(F)::v, n = 4 - f;
      sfor<decltype(A)::v, decltype(B)::v>([&](auto I) {
        constexpr int i = decltype(I)::v, pr = i / n, e = f + i % n;
        constexpr int xt = i < XL * n / 4 ? XT1 : XT;
        if constexpr (5 + i >= xt) vmwait<xt - 1>();
        if (i == XT1 - 1) { lds_set(lds, C_XO, j); STAMP(5); }
        if constexpr (XG && i == XL * n / 4) lds_wait(lds, C_HD, 4u);
        static_assert(XT1 <= XT && XT1 <= XL / 2, "");
        if (i == 3 * n + XT - 1) lds_set(lds, C_XA, j);
        if (i == 5 * n + XT - 1) { lds_set(lds, C_XB, j); STAMP(6); }
        dmai<L_X + xslot(pr) * 16384, pr * 128>(rx, xo, sb[e], ld[e]);
      });
      static_assert(XT <= 8, "");
    };
    if (j) stream(IC<0>{}, IC<0>{}, IC<36>{}); else stream(IC<2>{}, IC<0>{}, IC<18>{});
    STAMP(1);
    u32x4 x9[4];
#pragma unroll
    for (int i = 0; i < 4; ++i) LDXI(x9[i], rx, xo, sb[i] + 9 * 128);
    if (j == 0) {
      lds_wait(lds, C_XO, ALL4);
      const int xa = L_X + (2 * m0 + (xcd & 1)) * 1024 + l16;
      u32x4 xr[10];
#pragma unroll
      for (int p = 0; p < 10; ++p) xr[p] = *(const u32x4*)(lds + (p < 9 ? xa + p * 16384 : L_X9 + (xcd & 1) * 1024 + l16));
      float s0 = 0.f, s1 = 0.f, q0 = 0.f, q1 = 0.f;
#pragma unroll
      for (int p = 0; p < 10; ++p) {
        s0 = dot2(xr[p].x, 0x3f803f80u, s0); q0 = dot2(xr[p].x, xr[p].x, q0); s1 = dot2(xr[p].y, 0x3f803f80u, s1); q1 = dot2(xr[p].y, xr[p].y, q1);
        s0 = dot2(xr[p].z, 0x3f803f80u, s0); q0 = dot2(xr[p].z, xr[p].z, q0); s1 = dot2(xr[p].w, 0x3f803f80u, s1); q1 = dot2(xr[p].w, xr[p].w, q1);
      }
      const float mean = sum8(s0 + s1) * (1.0f / DM);
      float var = sum8(q0 + q1) * (1.0f / DM) - mean * mean;
      if (__builtin_expect(__any(mean * mean > 16.0f * var), 0)) {
        float q2 = 0.f;
#pragma unroll
        for (int p = 0; p < 10; ++p)
#pragma unroll
          for (int i = 0; i < 4; ++i) {
            const float d0 = dot2(xr[p][i], ONE_LO, -mean), d1 = dot2(xr[p][i], ONE_HI, -mean);
            q2 += d0 * d0; q2 += d1 * d1;
          }
        var = sum8(q2) * (1.0f / DM);
      }
      ST64(((u32x2){pack2(mean, __builtin_amdgcn_sqrtf(rbf(rbf(var) + 1e-05f))), tag32}), (l & 7) ? OOB : WS_STAT + rc * 64 + l);
    }
    vmwait<4>();
    lds_set(lds, C_XC, j);
    STAMP(4);
    if (HP) __builtin_amdgcn_s_setprio(HP);
    lds_wait(lds, CBG ? C_CH : C_UI, ALL4);
    STAMP(16);
    {
      u32x4 g[4];
      u32x2 sg, fp;
#pragma unroll
      for (int q = 0; q < 4; ++q) LDX(g[q], cbo, q * 32768);
      asm volatile("buffer_load_dwordx2 %0, %1, %2, 0 offen sc1" : "=&v"(sg) : "v"(sgo), "s"(rs) : "memory");
      if (!CBG) lds_wait(lds, C_CH, ALL4);
      asm volatile("buffer_load_dwordx2 %0, %1, %2, 0 offen sc1" : "=&v"(fp) : "v"(fpo), "s"(rs) : "memory");
      vmwait<6>();
#pragma unroll
      for (int i = 0; i < 4; ++i) { READY(x9[i]); *(u32x4*)(lds + x9a[i]) = x9[i]; }
      SBAR();
      vmwait<1>();
      STAMP(19);
      READY(g[0]); READY(g[1]); READY(g[2]); READY(g[3]); READY(sg);
      RETRY4(g, cbo, 32768, 0);
      *(float*)(lds + cws) = rs32(SUM4(g, x), SUM4(g, z));
      if (__builtin_expect(!__all(sg.y == tag32), 0)) {
        int sp = 0;
        do { __builtin_amdgcn_s_sleep(2); sg = ld64(rs, sgo); } while (!__all(sg.y == tag32) && ++sp < SPIN_CAP);
      }
      const f32x2 ms = unpk(sg.x);
      const float r = __builtin_amdgcn_rcpf(ms.y);
      *(f32x2*)(lds + sws) = (f32x2){r, -ms.x * r};
      CFENCE();
      lds_set(lds, C_X9, j);
      STAMP(17);
      vmwait<0>();
      READY(fp);
      if (__builtin_expect(__builtin_amdgcn_ballot_w64(__builtin_bit_cast(u64, fp) != __builtin_bit_cast(u64, flagv)) != 0ull, 0)) wait_flag8(rs, fpo, flagv);
      lds_set(lds, C_FP, j);
      if (HP) __builtin_amdgcn_s_setprio(0);
      STAMP(21);
    }
    lds_wait(lds, C_FP, ALL4);
#define WOLT(a, b) do { _Pragma("unroll") for (int i = (a); i < (b); ++i) { vmwait<TW2 - 1>(); LDXO(wov[i], l16, wob0 + (i >> 2) * 4096, (i & 3) * 1024); } } while (0)
#pragma unroll
    for (int i = 0; i < WP; ++i) { vmwait<TW - 1>(); LDXO(wov[i], l16, wob0 + (i >> 2) * 4096, (i & 3) * 1024); }
    lds_wait(lds, C_KV, ALL4);
    lds_wait(lds, C_HL, ALL4);
    STAMP(27);
    WOLT(WP, WA);
    sfor<0, 4>([&](auto I) { constexpr int i = decltype(I)::v; dmai<i * 1024, 0>(rl, lsrc, lso + i * 8 * 7680, lbase + L_LT(j)); });
    if (PTX == 0) TOUCH();
    WOLT(WA, WB);
    vmwait<WB - WA>();
    STAMP(24);
    if (LPR) __builtin_amdgcn_s_setprio(LPR);
    {
      f32x4 d = {0.f, 0.f, 0.f, 0.f};
#pragma unroll
      for (int i = 0; i < 4; ++i) {
        const s16x4 lo = trread(lds + lta + i * 32), hi = trread(lds + lta + i * 32 + 512);
        d = __builtin_amdgcn_mfma_f32_16x16x32_bf16(__builtin_shufflevector(lo, hi, 0, 1, 2, 3, 4, 5, 6, 7), __builtin_bit_cast(bf16x8, lb[i]), d, 0, 0, 0);
      }
      ST128(((u32x4){__float_as_uint(me ? d.x : d.z), tag32, __float_as_uint(me ? d.y : d.w), tag32}), ldst);
      SBAR();
    }
    if (LPR) __builtin_amdgcn_s_setprio(0);
    STAMP(22);
#pragma unroll
    for (int i = WB; i < NA; ++i) LDXO(wov[i], l16, wob0 + (i >> 2) * 4096, (i & 3) * 1024);
    if (PTX == 1) TOUCH();
    if (j == 0) {
      lds_wait(lds, C_GO, 1u);
#pragma unroll
      for (int i = 0; i < 5; ++i) LDX(hv[i], hlo, i * 1024);
      if (PTX == 2) lds_set(lds, C_GO, 0 + 4);
    }
    if (PTX == 2) { lds_wait(lds, C_GO + 1, 1u); TOUCH(); }
    STAMP(18);
  }
  STAMP(23);
  asm volatile("" : "+v"(t));
  l = t & 63; g4 = l >> 4; i16 = l & 15;
  LBAR();
  int xro = w < 4 ? ((rg * 4 + w) * 16 + i16) * 1280 + cg * 128 + g4 * 16 : OOB, xre = w < 4 ? ((rg * 4 + w) * 16 + i16) * 1280 + 1024 + cg * 32 + g4 * 8 : OOB;
  u32x4 xa, xb;
  u32x2 xc;
#define X_LOAD() do { \
    LDXI(xa, rx, xro, 0); LDXI(xb, rx, xro, 64); \
    asm volatile("buffer_load_dwordx2 %0, %1, %2, 0 offen" : "=&v"(xc) : "v"(xre), "s"(rx) : "memory"); \
  } while (0)
  if (XP == 0) X_LOAD();
  const bool red = w == RW && b >= 168 && b < 208;
  u32x4 mg[10];
  int mgo;
  if (red) {
    mgo = WS_MODP + (l >> 5) * 30720 + ((b - 148) * 64 + (l & 31) * 2) * 8;
#pragma unroll
    for (int q = 0; q < 10; ++q) LDX(mg[q], mgo, q * 61440);
  }
  if (w == 4) {
    vmwait<(PTX == 2 ? 1 : 0) + (XP == 0 ? 3 : 0)>();
#pragma unroll
    for (int i = 0; i < 5; ++i) READY(hv[i]);
    unsigned bad = 0u;
#pragma unroll
    for (int i = 0; i < 5; ++i) bad |= off2(hv[i], tag32);
    if (__builtin_expect(rb > 0 && !none(bad), 0)) {
      int sp = 0;
      do {
        __builtin_amdgcn_s_sleep(1);
#pragma unroll
        for (int i = 0; i < 5; ++i) LDX(hv[i], hlo, i * 1024);
        vmwait<0>();
        bad = 0u;
#pragma unroll
        for (int i = 0; i < 5; ++i) { READY(hv[i]); bad |= (hv[i].y ^ tag32) | (hv[i].w ^ tag32); }
      } while (!__all(bad == 0u) && ++sp < SPIN_CAP);
    }
#pragma unroll
    for (int i = 0; i < 5; ++i) *(u32x2*)(lds + L_KV + i16 * KVS + g4 * 8 + i * 32) = (u32x2){hv[i].x, hv[i].z};
    CFENCE();
  }
  SBAR();
  STAMP(7);

  {
    const char* kb = lds + kbo;
    f32x4 sc[2] = {mbias, mbias};
    bf16x8 vf[2][2];
    {
      typedef __attribute__((ext_vector_type(6))) unsigned u32x6;
      u32x6 kk[2];
      u32x4 qb[2][2];
#pragma unroll
      for (int par = 0; par < 2; ++par) {
        const u32x2 k0 = *(const u32x2*)(kb + par * KVS), k1 = *(const u32x2*)(kb + par * KVS + 32), k2 = *(const u32x2*)(kb + par * KVS + 64);
        kk[par] = (u32x6){k0.x, k0.y, k1.x, k1.y, k2.x, k2.y};
#pragma unroll
        for (int h = 0; h < 2; ++h) {
          const u32x2 a = *(const u32x2*)(lds + qa[par][2 * h]), c = *(const u32x2*)(lds + qa[par][2 * h + 1]);
          qb[h][par] = (u32x4){a.x, a.y, c.x, c.y};
        }
      }
      const char* vb = lds + vbo;
#pragma unroll
      for (int h = 0; h < 2; ++h)
#pragma unroll
        for (int d = 0; d < 2; ++d) {
          const s16x4 lo = trread(vb + h * 40 + d * 32), hi = trread(vb + KVS + h * 40 + d * 32);
          vf[h][d] = __builtin_shufflevector(lo, hi, 0, 1, 2, 3, 4, 5, 6, 7);
        }
      SBAR();
      asm volatile("" : "+v"(kk[0]), "+v"(kk[1]));
#pragma unroll
      for (int par = 0; par < 2; ++par) {
        sc[0] = __builtin_amdgcn_mfma_f32_16x16x32_bf16(__builtin_bit_cast(bf16x8, __builtin_shufflevector(kk[par], kk[par], 0, 1, 2, 3)),
                                                        __builtin_bit_cast(bf16x8, qb[0][par]), sc[0], 0, 0, 0);
        sc[1] = __builtin_amdgcn_mfma_f32_16x16x32_bf16(__builtin_bit_cast(bf16x8, __builtin_shufflevector(kk[par], kk[par], 2, 3, 4, 5)),
                                                        __builtin_bit_cast(bf16x8, qb[1][par]), sc[1], 0, 0, 0);
      }
    }
    f32x4 od[2][2], lg[2];
    float mx[2], sm[2];
#pragma unroll
    for (int h = 0; h < 2; ++h) {
      lg[h] = sc[h];
      mx[h] = vmax(fmaxf(fmaxf(sc[h].x, sc[h].y), sc[h].z), sc[h].w);
    }
    rows2<true>(mx[0], mx[1]);
#pragma unroll
    for (int h = 0; h < 2; ++h) {
      const float k2 = 0.22360679774997896f * 1.4426950408889634f;
      const float nm = mx[h] * -k2;
#pragma unroll
      for (int j = 0; j < 4; ++j) lg[h][j] = __builtin_amdgcn_exp2f(__builtin_fmaf(lg[h][j], k2, nm));
      sm[h] = (lg[h][0] + lg[h][1]) + (lg[h][2] + lg[h][3]);
    }
    rows2<false>(sm[0], sm[1]);
#pragma unroll
    for (int h = 0; h < 2; ++h) {
      const float inv = __builtin_amdgcn_rcpf(sm[h]);
      const unsigned p01 = pack2(lg[h][0] * inv, lg[h][1] * inv), p23 = pack2(lg[h][2] * inv, lg[h][3] * inv);
      const bf16x8 pf = __builtin_bit_cast(bf16x8, ((u32x4){p01 & me, p23 & me, p01 & ~me, p23 & ~me}));
#pragma unroll
      for (int d = 0; d < 2; ++d)
        od[h][d] = __builtin_amdgcn_mfma_f32_16x16x32_bf16(vf[h][d], pf, (f32x4){0.f, 0.f, 0.f, 0.f}, 0, 0, 0);
    }
    ST128(((u32x4){pack2v(od[0][0].xy), pack2v(od[0][0].zw), pack2v(od[1][0].xy), pack2v(od[1][0].zw)}), odst);
    ST128(((u32x4){pack2v(od[0][1].xy), pack2v(od[0][1].zw), pack2v(od[1][1].xy), pack2v(od[1][1].zw)}), odst4);
    SBAR();
    if (XP == 1) X_LOAD();
    STAMP(25);
    int woa = L_WO + (w - 4) * 25600 + l * 16;
    asm volatile("" : "+v"(woa));
    constexpr int NA1 = NA < 13 ? NA : 13;
    if (w >= 4) {
      vmwait<2 + NA - NA1>();
#pragma unroll
      for (int i = 0; i < NA1; ++i) READY(wov[i]);
#pragma unroll
      for (int i = 0; i < NA1; ++i) *(u32x4*)(lds + woa + i * 1024) = wov[i];
    }
    int fo = l == 0 ? WS_FLAGO + (rb * 8 + mi) * 128 + hp * 8 : OOB;
    asm volatile("" : "+v"(fo));
    DRAIN();
    ST64(flagv, fo);
    SBAR();
    lds_set(lds, C_AD, w);
    if (XP == 2 && w < 4) X_LOAD();
    if (w >= 4) sfor<NA, 25>([&](auto I) { constexpr int i = decltype(I)::v; dmas<L_WO + i * 1024, (i & 3) * 1024>(rs, l * 16, WS_WOT + cg * 102400 + (w - 4) * 25600 + (i >> 2) * 4096, lbase + (w - 4) * 25600); });
    if (red) {
      unsigned bad = 0u;
#pragma unroll
      for (int q = 0; q < 10; ++q) { READY(mg[q]); bad |= off2(mg[q], tag32); }
      if (__builtin_expect(!none(bad), 0)) {
        int sp = 0;
        do {
          __builtin_amdgcn_s_sleep(2);
          bad = 0u;
#pragma unroll
          for (int q = 0; q < 10; ++q) mg[q] = ld128(rs, mgo + q * 61440);
#pragma unroll
          for (int q = 0; q < 10; ++q) bad |= off2(mg[q], tag32);
        } while (!none(bad) && ++sp < SPIN_CAP);
      }
      f32x2 s = {0.f, 0.f};
#pragma unroll
      for (int q = 0; q < 10; ++q) s += (f32x2){__uint_as_float(mg[q].x), __uint_as_float(mg[q].z)};
      s = rbf2((f32x2){swapsum32(s.x), swapsum32(s.y)});
      if (b >= 188 && b < 198) s = rbf2(s + (f32x2){1.0f, 1.0f});
      ST128(((u32x4){__float_as_uint(s.x), tag32, __float_as_uint(s.y), tag32}), l < 32 ? WS_MODF + ((b - 168) * 64 + l * 2) * 8 : OOB);
    }
    if (w >= 4) {
#pragma unroll
      for (int i = NA1; i < NA; ++i) READY(wov[i]);
#pragma unroll
      for (int i = NA1; i < NA; ++i) *(u32x4*)(lds + woa + i * 1024) = wov[i];
      if (NA < 25) DRAIN();
      CFENCE();
      lds_set(lds, C_WL, w - 4);
    }
  }
  STAMP(8);
  const __amdgpu_buffer_rsrc_t ro = __builtin_amdgcn_make_buffer_rsrc((void*)out, 0, S_LEN * DM * 2, 0x00020000);
  u32x4 dw[4];
#define DW_LOAD() do { \
    const __amdgpu_buffer_rsrc_t rd = __builtin_amdgcn_make_buffer_rsrc((void*)wd, 0, FF * 1280, 0x00020000); \
    const int o = w >= 3 ? (fb * 16 + g4 * 4) * 1280 + (w - 3) * 256 + i16 * 16 : OOB; \
    _Pragma("unroll") for (int i = 0; i < 4; ++i) dw[i] = __builtin_amdgcn_raw_buffer_load_b128(rd, o, i * 1280, 0); \
    SBAR(); \
  } while (0)
  bf16x8 af[5];
  unsigned dp[2][8];
  int hbo, mro, ydst, bdst;
  unsigned x1[10];
#define MLP_PREP() do { \
    hbo = L_H2 + (w & 3) * 64 + g4 * 16; mro = i16 == 0 ? L_MRED + w * 64 + g4 * 16 : L_MRED + 1024; \
    ydst = w >= 3 ? WS_YP + fb * 5120 + (w - 3) * 1024 + l * 16 : OOB; \
    if (PW & 4) { ST128(((u32x4){0u, ~tag32, 0u, ~tag32}), ydst); SBAR(); } \
    lds_wait(lds, C_GL, ALL4); \
    const char* stg = lds + L_GU + (kh * 20 + m2) * 1024 + g4 * 256 + i16 * 8; \
    _Pragma("unroll") for (int i = 0; i < 5; ++i) { \
      const s16x4 lo = trread(stg + i * 4096), hi = trread(stg + i * 4096 + 128); \
      af[i] = __builtin_shufflevector(lo, hi, 0, 1, 2, 3, 4, 5, 6, 7); \
      asm volatile("" : "+v"(af[i])); \
    } \
    _Pragma("unroll") for (int p = 0; p < 2; ++p) \
      _Pragma("unroll") for (int k = 0; k < 4; ++k) { \
        dp[p][2 * k] = __builtin_amdgcn_perm(dw[2 * p + 1][k], dw[2 * p][k], 0x05040100u); \
        dp[p][2 * k + 1] = __builtin_amdgcn_perm(dw[2 * p + 1][k], dw[2 * p][k], 0x07060302u); \
        asm volatile("" : "+v"(dp[p][2 * k]), "+v"(dp[p][2 * k + 1])); \
      } \
    asm volatile("" : "+v"(hbo), "+v"(mro), "+v"(ydst)); \
  } while (0)
  if (opq(w) < 4) {
    const int mt = rg * 4 + w;
    int wq0 = L_WO + l * 16, wq1 = wq0 + 51200, ofo = WS_FLAGO + mt * 128 + i16 * 8, l16 = l * 16, l8 = l * 8;
    int pao = L_PAR + g4 * 80, puo = L_PAR + 1280 + g4 * 48;
    int kdst = l == 0 ? WS_KEY + (cg * 128 + mt) * KS : OOB;
    bdst = WS_BEST + mt * 5120 + cg * 320 + g4 * 16;
    unsigned rowk = 2047 - (mt * 16 + i16);
    const int osrc = opq(WS_O + mt * 20480), ssrc = opq(WS_ST2 + mt * 4096);
    asm volatile("" : "+v"(xro), "+v"(xre), "+v"(wq0), "+v"(wq1), "+v"(ofo), "+v"(l16), "+v"(l8), "+v"(pao), "+v"(puo));
    asm volatile("" : "+v"(kdst), "+v"(bdst), "+v"(rowk));
    if (XP == 2) DRAIN();
    READY(xa); READY(xb); READY(xc);
    f32x4 xf[5];
    xf[0].xy = unpk(xa.x); xf[0].zw = unpk(xa.y); xf[1].xy = unpk(xa.z); xf[1].zw = unpk(xa.w);
    xf[2].xy = unpk(xb.x); xf[2].zw = unpk(xb.y); xf[3].xy = unpk(xb.z); xf[3].zw = unpk(xb.w);
    xf[4].xy = unpk(xc.x); xf[4].zw = unpk(xc.y);
    asm volatile("" : "+v"(xf[0]), "+v"(xf[1]), "+v"(xf[2]), "+v"(xf[3]), "+v"(xf[4]));
    SBAR();
    lds_wait(lds, C_OF, ALL4);
    if (ODL) __builtin_amdgcn_s_sleep(ODL);
    wait_flag8(rs, ofo, flagv);
    STAMP(9);
    u32x4 of[20];
#pragma unroll
    for (int i = 0; i < O1; ++i) { of[i] = __builtin_amdgcn_raw_buffer_load_b128(rs, l16 + (i & 3) * 1024, osrc + (i >> 2) * 4096, SC1); SBAR(); }
    lds_wait(lds, C_WL, ALL4);
    bf16x8 wq[QW];
    auto wfr = [&](int f) __attribute__((always_inline)) { return *(const bf16x8*)(lds + (f < 50 ? wq0 + f * 1024 : wq1 + (f - 50) * 1024)); };
#pragma unroll
    for (int i = 0; i < QW; ++i) wq[i] = wfr(i);
    SBAR();
    f32x4 a2[5], ga[5];
    u32x4 ua, ub, uc;
    unsigned rdy, rdys;
    sfor<0, 100>([&](auto F) {
      constexpr int f = decltype(F)::v, i = f / 5, n = f % 5;
      if constexpr (f == 100 - QW) {
        if (__builtin_expect(rdys != ALL4, 0)) lds_wait(lds, C_PR, ALL4);
        CFENCE();
      }
      if (i == 0) MFMA0(a2[n], wq[f % QW], __builtin_bit_cast(bf16x8, of[i])); else MFMA(a2[n], wq[f % QW], __builtin_bit_cast(bf16x8, of[i]));
      SBAR();
      if constexpr (f + QW < 100) wq[f % QW] = wfr(f + QW);
      if constexpr (f % OG == 0 && O1 + f / OG < 20) {
        constexpr int k = O1 + f / OG;
        of[k] = __builtin_amdgcn_raw_buffer_load_b128(rs, l16 + (k & 3) * 1024, osrc + (k >> 2) * 4096, SC1);
      }
      if constexpr (f == (20 - O1) * OG) lds_set(lds, C_OI, w);
      if constexpr (f == 92 - QW) rdy = lds_cnt(lds, C_PR);
      if constexpr (f == 98 - QW) rdys = __builtin_amdgcn_readfirstlane(rdy);
      constexpr int e = f - (100 - QW);
      if constexpr (e >= 0 && e < 5) ga[e] = *(const f32x4*)(lds + pao + e * 16);
      if constexpr (e == 5) ua = *(const u32x4*)(lds + puo);
      if constexpr (e == 6) ub = *(const u32x4*)(lds + puo + 16);
      if constexpr (e == 7) uc = *(const u32x4*)(lds + puo + 32);
      SBAR();
    });
    STAMP(26);
    float s = 0.f, q = 0.f, d = 0.f;
    {
      const unsigned uh[10] = {ua.x, ua.y, ua.z, ua.w, ub.x, ub.y, ub.z, ub.w, uc.x, uc.y};
#pragma unroll
      for (int n = 0; n < 5; ++n) {
        if (n == 0) asm volatile("s_nop 1" : "+v"(a2[0]));
        if (n == 4) asm volatile("" : "+v"(a2[4]));
        xf[n] = ga[n] * a2[n] + xf[n];
        x1[2 * n] = pack2v(xf[n].xy); x1[2 * n + 1] = pack2v(xf[n].zw);
#pragma unroll
        for (int k = 2 * n; k < 2 * n + 2; ++k) { s = dot2(x1[k], 0x3f803f80u, s); q = dot2(x1[k], x1[k], q); d = dot2(x1[k], uh[k], d); }
        SBAR();
      }
    }
    __builtin_amdgcn_raw_buffer_store_b64(((u32x2){__float_as_uint(rs16(rs32(s, q), rs32(d, __uint_as_float(uc.z)))), tag32}), rs, l8, ssrc + cg * 512, SAUX);
    SBAR();
    STAMP(10);
    f32x4 sb[5], c1[5], gb[5];
#pragma unroll
    for (int n = 0; n < 5; ++n) {
      sb[n] = *(const f32x4*)(lds + pao + 320 + n * 16); c1[n] = *(const f32x4*)(lds + pao + 640 + n * 16); gb[n] = *(const f32x4*)(lds + pao + 960 + n * 16);
    }
#pragma unroll
    for (int n = 0; n < 5; ++n) asm volatile("" : "+v"(sb[n]), "+v"(c1[n]), "+v"(gb[n]));
    auto gather = [&](int so) __attribute__((always_inline)) {
      u32x2 p[8];
      int sp = 0;
      for (;;) {
        asm volatile("" : "+v"(l8));
#pragma unroll
        for (int c = 0; c < 8; ++c) p[c] = __builtin_amdgcn_raw_buffer_load_b64(rs, l8 + c * 512, so, GAUX);
        SBAR();
        lds_set(lds, C_SI, w);
        unsigned bad = 0u;
#pragma unroll
        for (int c = 0; c < 8; c += 2) bad |= off2((u32x4){0u, p[c].y, 0u, p[c + 1].y}, tag32);
        if (none(bad) || ++sp > SPIN_CAP) break;
        __builtin_amdgcn_s_sleep(SRS);
      }
      return add1(add1(add1(__uint_as_float(p[0].x), __uint_as_float(p[1].x)), add1(__uint_as_float(p[2].x), __uint_as_float(p[3].x))),
                  add1(add1(__uint_as_float(p[4].x), __uint_as_float(p[5].x)), add1(__uint_as_float(p[6].x), __uint_as_float(p[7].x))));
    };
    if (SDR) DRAIN();
    if (SDL) __builtin_amdgcn_s_sleep(SDL);
    float U;
    {
      const unsigned v = __float_as_uint(gather(ssrc));
      STAMP(11);
      auto r = __builtin_amdgcn_permlane16_swap(v, v, false, false);
      auto r0 = __builtin_amdgcn_permlane32_swap(r[0], r[0], false, false), r1 = __builtin_amdgcn_permlane32_swap(r[1], r[1], false, false);
      s = __uint_as_float(r0[0]) * (1.0f / DM); q = __uint_as_float(r0[1]); d = __uint_as_float(r1[0]); U = __uint_as_float(r1[1]);
    }
    float var = q * (1.0f / DM) - s * s;
    if (__builtin_expect(__any(s * s > 16.0f * var), 0)) {
      float q2 = 0.f;
#pragma unroll
      for (int k = 0; k < 10; ++k) {
        const float e0 = dot2(x1[k], ONE_LO, -s), e1 = dot2(x1[k], ONE_HI, -s);
        q2 += e0 * e0; q2 += e1 * e1;
      }
      __builtin_amdgcn_raw_buffer_store_b64(((u32x2){__float_as_uint(q2), tag32}), rs, l8, ssrc + 128 * 4096 + cg * 512, SC1);
      var = swapsum32(swapsum16(gather(ssrc + 128 * 4096))) * (1.0f / DM);
    }
    const float mb = rbf(s);
    const float isd = __builtin_amdgcn_rcpf(rbf(__builtin_amdgcn_sqrtf(rbf(rbf(var) + 1e-05f))));
    unsigned key, kmx;
    {
      const float r = (d - mb * U) * isd;
      const unsigned u = __float_as_uint(r);
      unsigned ord = u ^ ((unsigned)((int)u >> 31) | 0x80000000u);
      if (r != r) ord = 0u;
      key = (ord & ~2047u) | rowk;
      kmx = umax(key, dppu<0xB1>(key)); kmx = umax(kmx, dppu<0x4E>(kmx)); kmx = umax(kmx, dppu<0x141>(kmx)); kmx = umax(kmx, dppu<0x140>(kmx));
    }
    ST64(((u32x2){kmx, tag32}), kdst);
    SBAR();
    lds_set(lds, C_KP, w);
    STAMP(12);
    if (key != kmx) bdst = OOB;
    const float nmi = -mb * isd;
    unsigned oq[10];
#pragma unroll
    for (int n = 0; n < 5; ++n) {
      const f32x4 h = (xf[n] * isd + nmi) * c1[n] + sb[n];
      __builtin_amdgcn_raw_buffer_store_b128(((u32x4){pack2v(h.xy), tag32, pack2v(h.zw), tag32}), rs, bdst, n * 64, SC1);
      sb[n] = h;
    }
    SBAR();
#pragma unroll
    for (int n = 0; n < 5; ++n) {
      const f32x4 o = gb[n] * sb[n] + xf[n];
      oq[2 * n] = pack2v(o.xy); oq[2 * n + 1] = pack2v(o.zw);
    }
    __builtin_amdgcn_raw_buffer_store_b128(((u32x4){oq[0], oq[1], oq[2], oq[3]}), ro, xro, 0, SC1);
    __builtin_amdgcn_raw_buffer_store_b128(((u32x4){oq[4], oq[5], oq[6], oq[7]}), ro, xro, 64, SC1);
    __builtin_amdgcn_raw_buffer_store_b64(((u32x2){oq[8], oq[9]}), ro, xre, 0, SC1);
    SBAR();
#define X1_PUB() do { \
      DRAIN(); \
      _Pragma("unroll") for (int n = 0; n < 5; ++n) __builtin_amdgcn_raw_buffer_store_b128(((u32x4){x1[2 * n], tag32, x1[2 * n + 1], tag32}), rs, bdst, 2560 + n * 64, SC1); \
      SBAR(); \
    } while (0)
    if (is_gu) { DW_LOAD(); MLP_PREP(); }
    if (!X1L || !is_gu) X1_PUB();
    STAMP(20);
    if (!is_gu) { STAMP_DUMP(); return; }
  } else {
    const int j = w - 4;
    const int pc = l < 32 ? cg * 64 + 2 * l : 512 + cg * 16 + 2 * min(l - 32, 7);
    const int pi = l < 32 ? ((l >> 2) & 3) * 20 + (l >> 4) * 8 + (l & 3) * 2 : (((l - 32) >> 1) & 3) * 20 + 16 + (l & 1) * 2;
    int pgo = WS_MODF + (j * 640 + pc) * 8, pw = l < 40 ? L_PAR + j * 320 + pi * 4 : L_DUM;
    int pwu = l < 40 ? L_PAR + 1280 + (pi / 20) * 48 + (pi % 20) * 2 : L_DUM;
    const __amdgpu_buffer_rsrc_t rwr = __builtin_amdgcn_make_buffer_rsrc((void*)wr, 0, DM * 2, 0x00020000);
    unsigned wrp;
    LDXW(wrp, rwr, l < 40 ? pc * 2 : OOB, 0);
    const __amdgpu_buffer_rsrc_t rgu = __builtin_amdgcn_make_buffer_rsrc((void*)(j < 2 ? wg : wu), 0, DM * FF * 2, 0x00020000);
    int guo = (l >> 1) * (FF * 2) + (l & 1) * 16;
    const int gus = opq((j & 1) * 320 * FF * 2 + fb * 32);
    asm volatile("" : "+v"(pgo), "+v"(pw), "+v"(pwu), "+v"(guo));
#define GU(a, b) do { if (is_gu) sfor<(a), (b)>([&](auto I) { constexpr int i = decltype(I)::v; dmai<L_GU + i * 1024, 0>(rgu, guo, gus + i * 32 * FF * 2, lbase + j * 10240); }); } while (0)
    if (is_gu) { lds_wait(lds, C_AD, ALL4); lds_wait(lds, C_OF, ALL4); }
    {
      u32x4 pg;
      if (PDL) __builtin_amdgcn_s_sleep(PDL);
      LDX(pg, pgo, 0);
      GU(0, G1N);
      if (is_gu) vmwait<G1N>(); else vmwait<0>();
      READY(pg); READY(wrp);
      if (__builtin_expect(!none(off2(pg, tag32)), 0)) {
        int sp = 0;
        do { __builtin_amdgcn_s_sleep(4); pg = ld128(rs, pgo); } while (!none(off2(pg, tag32)) && ++sp < SPIN_CAP);
      }
      *(u32x2*)(lds + pw) = (u32x2){pg.x, pg.z};
      if (j == 2) {
        const unsigned uh = pack2v((f32x2){__uint_as_float(pg.x), __uint_as_float(pg.z)} * unpk(wrp));
        *(unsigned*)(lds + pwu) = uh;
        const float us = sum64(dot2(uh, 0x3f803f80u, 0.f)) * 0.25f;
        *(float*)(lds + L_PAR + 1280 + g4 * 48 + 40) = us;
      }
      CFENCE();
      lds_set(lds, C_PR, j);
    }
    STAMP(9);
    if (TK < 10 && slot >= 16 && j * 8 + xcd < 27) {
      constexpr int NR = 4 * (10 - TK);
      const int gq = j * 8 + xcd, r = (slot - 16) * NR + min(l & 31, NR - 1);
      const char* p0 = (const char*)(l < 32 ? wg : wu) + (r < 8 * NR ? r + 32 * TK : r + 64 * TK) * FF * 2 + gq * 128;
      const char* p1 = (const char*)wd + (gq * 640 + (slot - 16) * 40 + min(l, 39)) * 128;
      asm volatile("" : "+v"(p0), "+v"(p1));
      lds_wait(lds, T2G, ALL4);
      asm volatile("s_mov_b32 m0, %2\n\ts_nop 0\n\tglobal_load_lds_dword %0, off\n\tglobal_load_lds_dword %1, off" :: "v"(p0), "v"(p1), "s"(lbase + L_DUM) : "m0", "memory");
      static_assert(NR <= 32, "");
    }
    if (is_gu) {
      if (G2N > G1N) lds_wait(lds, C_OI, ALL4);
      GU(G1N, G2N);
      lds_wait(lds, C_SI, ALL4);
      GU(G2N, 10);
      DW_LOAD();
      vmwait<4>();
      lds_set(lds, C_GL, j);
      MLP_PREP();
    } else {
      if (!is_dn) { DRAIN(); STAMP_DUMP(); return; }
      const int col = (dnb >> 3) * 128 + (dnb & 1) * 64 + (l & 7) * 8 + ((dnb >> 1) & 3) * 2 + (l >> 5);
      float V, gbv;
      {
        u32x4 g[5];
        u32x2 gg;
        unsigned wv[5];
        int sp = 0;
#pragma unroll
        for (int i = 0; i < 5; ++i) wv[i] = __builtin_amdgcn_raw_buffer_load_b32(rwr, l * 4, i * 256, 0);
        for (;;) {
          unsigned bad = 0u;
#pragma unroll
          for (int i = 0; i < 5; ++i) g[i] = ld128(rs, WS_MODF + 5120 + i * 1024 + l * 16);
          gg = ld64(rs, WS_MODF + (1920 + col) * 8);
#pragma unroll
          for (int i = 0; i < 5; ++i) bad |= off2(g[i], tag32);
          if (none(bad | (gg.y ^ tag32)) || ++sp > SPIN_CAP) break;
          __builtin_amdgcn_s_sleep(4);
        }
        V = 0.f;
#pragma unroll
        for (int i = 0; i < 5; ++i) { const f32x2 f = unpk(wv[i]); V += __uint_as_float(g[i].x) * f.x; V += __uint_as_float(g[i].z) * f.y; }
        V = sum64(V);
        gbv = __uint_as_float(gg.x);
      }
      if (j == 0) {
        u32x4 kk;
        int sp = 0;
        lds_wait(lds, C_KP, ALL4);
        __builtin_amdgcn_s_sleep(KN);
        for (;;) {
          kk = keys(rs, xcd, l);
          if (none(off2(kk, tag32)) || ++sp > SPIN_CAP) break;
          __builtin_amdgcn_s_sleep(6);
        }
        const unsigned kmax = __builtin_amdgcn_readfirstlane(max64(umax(kk.x, kk.z)));
        const int sel = 2047 - (int)(kmax & 2047u);
        const int bo = col < 512 ? (col >> 6) * 320 + (((col >> 5) & 1) * 2 + ((col >> 2) & 1)) * 64 + ((col >> 3) & 3) * 16
                                 : ((col - 512) >> 4) * 320 + 256 + ((col >> 2) & 3) * 16;
        u32x2 rv;
        sp = 0;
        for (;;) {
          rv = ld64(rs, WS_BEST + (sel >> 4) * 5120 + ((l >> 3) & 1) * 2560 + bo + ((col >> 1) & 1) * 8);
          if (none(rv.y ^ tag32) || ++sp > SPIN_CAP) break;
          __builtin_amdgcn_s_sleep(1);
        }
        if (l < 16) *(unsigned*)(lds + L_H2 + l * 4) = rv.x;
        if (l == 0) *(unsigned*)(lds + L_MISC + 192) = kmax;
        CFENCE();
        lds_set(lds, C_SL, 0);
      } else lds_wait(lds, C_SL, 1u);
      STAMP(13);
      const unsigned kmax = *(const unsigned*)(lds + L_MISC + 192);
      const unsigned ord = (kmax & ~2047u) | 1024u;
      const float rsel = rbf(__uint_as_float((ord & 0x80000000u) ? (ord & 0x7fffffffu) : ~ord) + V);
      float h2 = bf2f(*(const u16*)(lds + L_H2 + (l & 7) * 4 + (l >> 5) * 2));
      float x1v = bf2f(*(const u16*)(lds + L_H2 + 32 + (l & 7) * 4 + (l >> 5) * 2));
      float sg = rbf(__builtin_amdgcn_rcpf(1.0f + __expf(-rsel)));
      int odo = (l & 24) == 0 ? ((2047 - (int)(kmax & 2047u)) * DM + col) * 2 : OOB;
      int yo = WS_YP + (27 * j + (l >> 3)) * 5120 + dnb * 128 + (l & 7) * 16, yo3 = yo + (l < 24 ? 24 * 5120 : 0);
      unsigned cnt = l == 0 ? 1u : 0u, ca = lbase + L_CNT + ((C_YD + l) & 63) * 4;
      int yw = L_MRED + l * 4;
      asm volatile("" : "+v"(h2), "+v"(x1v), "+v"(gbv), "+v"(sg), "+v"(odo), "+v"(cnt), "+v"(ca), "+v"(yw));
      u32x4 yv[4];
      int spins = 0;
      __builtin_amdgcn_s_sleep(YD);
      for (;;) {
        asm volatile("" : "+v"(yo), "+v"(yo3));
        yv[0] = LD128S(yo, 0); yv[1] = LD128S(yo, 8 * 5120); yv[2] = LD128S(yo, 16 * 5120); yv[3] = LD128S(yo3, 0);
        if (GOOD4(yv) || ++spins > SPIN_CAP) break;
        __builtin_amdgcn_s_sleep(YS);
      }
      STAMP(14);
      if (l >= 24) { yv[3].x = 0u; yv[3].z = 0u; }
      *(float*)(lds + yw + j * 256) = rs32(SUM4(yv, x), SUM4(yv, z));
      asm volatile("ds_add_rtn_u32 %0, %1, %0\n\ts_waitcnt lgkmcnt(0)" : "+v"(cnt) : "v"(ca) : "memory");
      if (__builtin_amdgcn_readfirstlane(cnt) == 3u) {
        const float* src = (const float*)(lds + yw);
        float v = swapsum16((src[0] + src[64]) + (src[128] + src[192]));
        v += dppf<0x128>(v);
        const float y = rbf(rbf(v) * sg);
        const float hh = rbf(h2 + y);
        __builtin_amdgcn_raw_buffer_store_b16((u16)(pack2(x1v + rbf(gbv * hh), 0.f) & 0xffffu), ro, odo, 0, SC1);
      }
      STAMP(15);
      STAMP_DUMP();
      return;
    }
  }

  {
    float z0 = 0.f, z1 = 0.f;
    STAMP(19);
    if (w == 7) {
      u32x4 kk;
      int sp = 0;
      int r0 = l * 16, r2 = 2048 + (l & 31) * 16, hw[3];
#pragma unroll
      for (int i = 0; i < 3; ++i) {
        const int e = i < 2 ? i * 64 + l : 128 + (l & 31), c = (e * 205) >> 12, n = (e - c * 20) >> 2, g = e & 3;
        hw[i] = L_H2 + (n < 4 ? c * 128 + (n >> 1) * 64 + g * 16 + (n & 1) * 8 : 1024 + c * 32 + g * 8);
        asm volatile("" : "+v"(hw[i]));
      }
      lds_wait(lds, C_KP, ALL4);
      if (KG) __builtin_amdgcn_s_sleep(KG);
      for (;;) {
        kk = keys(rs, xcd, l);
        if (none(off2(kk, tag32)) || ++sp > SPIN_CAP) break;
        __builtin_amdgcn_s_sleep(3);
      }
      const int sel = 2047 - (int)(__builtin_amdgcn_readfirstlane(max64(umax(kk.x, kk.z))) & 2047u);
      const int so = WS_BEST + (sel >> 4) * 5120;
      u32x4 r[3];
      sp = 0;
      for (;;) {
        asm volatile("" : "+v"(r0), "+v"(r2));
        r[0] = LD128S(r0, so); r[1] = LD128S(r0 + 1024, so); r[2] = LD128S(r2, so);
        if (none(off2(r[0], tag32) | off2(r[1], tag32) | off2(r[2], tag32)) || ++sp > SPIN_CAP) break;
        __builtin_amdgcn_s_sleep(1);
      }
#pragma unroll
      for (int i = 0; i < 3; ++i) *(u32x2*)(lds + hw[i]) = (u32x2){r[i].x, r[i].z};
    }
    if (PW & 4) DRAIN();
    LBAR();
    STAMP(13);
    {
      f32x4 ga = {0.f, 0.f, 0.f, 0.f};
#pragma unroll
      for (int i = 0; i < 5; ++i) ga = __builtin_amdgcn_mfma_f32_16x16x32_bf16(af[i], *(const bf16x8*)(lds + hbo + i * 256), ga, 0, 0, 0);
      *(f32x4*)(lds + mro) = ga;
      LBAR();
      if (w >= 3) {
        const float* src = (const float*)(lds + L_MRED) + g4 * 4 + (l & 3);
        const float gg = rbf((src[0] + src[16]) + (src[32] + src[48]));
        const float uu = rbf((src[64] + src[80]) + (src[96] + src[112]));
        const float sg = rbf(gg * __builtin_amdgcn_rcpf(1.0f + __expf(-gg)));
        const float act = sg * uu;
        const unsigned ap = pack2(act, dppf<0xB1>(act));
        const unsigned a01 = dppu<0x00>(ap), a23 = dppu<0xAA>(ap);
        float y[8];
#pragma unroll
        for (int j = 0; j < 8; ++j) y[j] = dot2(dp[1][j], a23, dot2(dp[0][j], a01, 0.f));
        const float u0 = rs32(y[0], y[4]), u1 = rs32(y[1], y[5]), u2 = rs32(y[2], y[6]), u3 = rs32(y[3], y[7]);
        z0 = rs16(u0, u2); z1 = rs16(u1, u3);
      }
    }
    ST128(((u32x4){__float_as_uint(z0), tag32, __float_as_uint(z1), tag32}), ydst);
    STAMP(14);
    if (X1L && w < 4) X1_PUB();
    if (b == 0 && t == 0) {
      const u64 ne = (((u64)ep1 << 32) | ep0) + 1ull;
      ST64(((u32x2){(unsigned)ne, (unsigned)(ne >> 32)}), WS_EPOCH);
    }
    STAMP_DUMP();
  }
}

extern "C" void kernel_launch(void* const* d_in, const int* in_sizes, int n_in, void* d_out, int out_size,
                              void* d_ws, size_t ws_size, hipStream_t stream) {
  k_all<<<256, 512, 0, stream>>>((const u16*)d_in[0], (const u16*)d_in[1], (const u16*)d_in[2], (const u16*)d_in[4],
                                 (const u16*)d_in[5], (const u16*)d_in[6], (const u16*)d_in[7], (const u16*)d_in[8],
                                 (const u16*)d_in[9], (char*)d_ws, (u16*)d_out);
}
```

```cpp
#include <hip/hip_runtime.h>
#include <stdint.h>

typedef __attribute__((ext_vector_type(8))) short bf16x8;
typedef __attribute__((ext_vector_type(4))) short s16x4;
typedef __attribute__((ext_vector_type(4))) float f32x4;
typedef __attribute__((ext_vector_type(2))) float f32x2;
typedef __attribute__((ext_vector_type(4))) unsigned u32x4;
typedef __attribute__((ext_vector_type(2))) unsigned u32x2;
typedef __attribute__((ext_vector_type(2))) __bf16 bf16x2;
typedef unsigned short u16;
typedef unsigned long long u64;
#define LAS __attribute__((address_space(3)))

#define GSL 2
#define WO1 4
#define TW 2
#define KS 8
#define S_LEN 2048
#define DM 640
#define FF 1728
#define SC1 16
#define YD 20
#define KN 30
#define YS 2
#define XS 5
#define PGI 9
#define O1 8
#define SPIN_CAP (1 << 18)
#define SENT 0xffffffffu
#define OOB 0x40000000

__device__ __forceinline__ float bf2f(unsigned v) { return __uint_as_float(v << 16); }
__device__ __forceinline__ unsigned pack2v(f32x2 v) {
  bf16x2 r = __builtin_convertvector(v, bf16x2);
  return __builtin_bit_cast(unsigned, r);
}
__device__ __forceinline__ unsigned pack2(float a, float b) { return pack2v((f32x2){a, b}); }
__device__ __forceinline__ float rbf(float f) { return __uint_as_float(pack2(f, 0.f) << 16); }
__device__ __forceinline__ f32x2 unpk(unsigned u) {
  f32x2 r;
  r.x = __uint_as_float(u << 16);
  r.y = __uint_as_float(u & 0xffff0000u);
  return r;
}
__device__ __forceinline__ f32x2 rbf2(f32x2 v) { return unpk(pack2v(v)); }
__device__ __forceinline__ unsigned nosent(unsigned u) { return u < 0xfffffffeu ? u : 0xfffffffeu; }
__device__ __forceinline__ unsigned umax(unsigned a, unsigned b) { return a > b ? a : b; }
__device__ __forceinline__ unsigned max4(u32x4 v, unsigned m) { return umax(umax(umax(v.x, v.y), v.z), umax(v.w, m)); }
__device__ __forceinline__ float dot2(unsigned a, unsigned b, float c) {
  return __builtin_amdgcn_fdot2_f32_bf16(__builtin_bit_cast(bf16x2, a), __builtin_bit_cast(bf16x2, b), c, false);
}

template <int CTRL> __device__ __forceinline__ float dppf(float v) {
  return __builtin_bit_cast(float, __builtin_amdgcn_update_dpp(0, __builtin_bit_cast(int, v), CTRL, 0xF, 0xF, true));
}
template <int CTRL> __device__ __forceinline__ unsigned dppu(unsigned v) {
  return (unsigned)__builtin_amdgcn_update_dpp(0, (int)v, CTRL, 0xF, 0xF, true);
}
__device__ __forceinline__ float swapsum16(float v) {
  auto r = __builtin_amdgcn_permlane16_swap(__float_as_uint(v), __float_as_uint(v), false, false);
  return __uint_as_float(r[0]) + __uint_as_float(r[1]);
}
__device__ __forceinline__ float swapsum32(float v) {
  auto r = __builtin_amdgcn_permlane32_swap(__float_as_uint(v), __float_as_uint(v), false, false);
  return __uint_as_float(r[0]) + __uint_as_float(r[1]);
}
__device__ __forceinline__ float sum64(float v) {
  v += dppf<0xB1>(v); v += dppf<0x4E>(v); v += dppf<0x141>(v); v += dppf<0x140>(v);
  return swapsum32(swapsum16(v));
}
__device__ __forceinline__ unsigned max64(unsigned v) {
  v = umax(v, dppu<0xB1>(v)); v = umax(v, dppu<0x4E>(v)); v = umax(v, dppu<0x141>(v)); v = umax(v, dppu<0x140>(v));
  auto r = __builtin_amdgcn_permlane16_swap(v, v, false, false);
  v = umax(r[0], r[1]);
  auto q = __builtin_amdgcn_permlane32_swap(v, v, false, false);
  return umax(q[0], q[1]);
}
typedef __attribute__((ext_vector_type(4))) short v4i16_t;
__device__ __forceinline__ s16x4 trread(const char* p) {
  return __builtin_bit_cast(s16x4, __builtin_amdgcn_ds_read_tr16_b64_v4i16((LAS v4i16_t*)p));
}
__device__ __forceinline__ u32x4 ld128(__amdgpu_buffer_rsrc_t rs, int off) {
  asm volatile("" : "+v"(off));
  return __builtin_amdgcn_raw_buffer_load_b128(rs, off, 0, SC1);
}
__device__ __forceinline__ u32x2 ld64(__amdgpu_buffer_rsrc_t rs, int off) {
  asm volatile("" : "+v"(off));
  return __builtin_amdgcn_raw_buffer_load_b64(rs, off, 0, SC1);
}
__device__ __forceinline__ unsigned ld32(__amdgpu_buffer_rsrc_t rs, int off) {
  asm volatile("" : "+v"(off));
  return __builtin_amdgcn_raw_buffer_load_b32(rs, off, 0, SC1);
}
#define ST128(v, off) __builtin_amdgcn_raw_buffer_store_b128((v), rs, (off), 0, SC1)
#define ST64(v, off) __builtin_amdgcn_raw_buffer_store_b64((v), rs, (off), 0, SC1)
#define ST32(v, off) __builtin_amdgcn_raw_buffer_store_b32((v), rs, (off), 0, SC1)
#define SBAR() __builtin_amdgcn_sched_barrier(0)
#define CFENCE() asm volatile("" ::: "memory")
#define LBAR() do { asm volatile("s_waitcnt lgkmcnt(0)" ::: "memory"); __builtin_amdgcn_s_barrier(); CFENCE(); } while (0)

#define STAMP_INIT() do {} while (0)
#define STAMP_DUMP() do {} while (0)
#define STAMP(i) do {} while (0)

#define LD128S(voff, soff) __builtin_amdgcn_raw_buffer_load_b128(rs, (voff), (soff), SC1)
#define DRAIN() asm volatile("s_waitcnt vmcnt(0)" ::: "memory")

#define WS_EPOCH 0
#define WS_FLAGP 4096
#define WS_FLAGW 8192
#define WS_FLAGO 20480
#define WS_STAT 40960
#define WS_MODF 69632
#define WS_MODP 131072
#define WS_CB (WS_MODP + 20 * 30720)
#define WS_WT (WS_CB + 8 * 16 * 2048)
#define WS_WOT (WS_WT + 16 * 20 * 8192)
#define WS_O (WS_WOT + 8 * 100 * 1024)
#define WS_HALO (WS_O + S_LEN * DM * 2)
#define WS_ST2 (WS_HALO + 256 * 5120)
#define WS_BEST (WS_ST2 + 2 * 128 * 4096)
#define WS_KEY (WS_BEST + 128 * 5120)
#define WS_YP (WS_KEY + 8 * 128 * KS)
#define WS_FO2 (WS_YP + 108 * 5120)
#define WS_END (WS_FO2 + 128 * 16 * 128)

#define L_MISC 0
#define L_CNT 256
#define L_MRED 512
#define L_DUM 2560
#define L_STG 4096
#define L_CS 4096
#define L_X9 14336
#define L_X 16384
#define L_LT(j) ((j) < 2 ? 6144 + (j) * 4096 : 45056 + (j) * 4096)
#define L_S 5120
#define KVS 248
#define L_KV 16384
#define L_GU 10240
#define L_WO 53248
#define L_XM 155648
#define L_XE 51200
#define L_H2 L_S
#define L_PAR 7680
#define C_XA 0
#define C_XB 1
#define C_XC 2
#define C_XO 3
#define C_HL 16
#define C_CH 17
#define C_PR 18
#define C_GO 8
#define C_OI 10
#define C_SI 11
#define C_GL 12
#define C_SL 13
#define C_KP 15
#define C_AD 24
#define C_K4 25
#define C_KD 28
#define C_LD 27
#define C_WL 26
#define C_YD 14
#define C_HD 20
#define C_KV 22
#define C_X9 4
#define C_FP 7
#define C_UI 6
__device__ __forceinline__ unsigned opaque(unsigned v) { asm("" : "+s"(v)); return v; }
#define ONE_LO opaque(0x00003f80u)
#define ONE_HI opaque(0x3f800000u)

__device__ __forceinline__ bool tag_ok(u32x4 g, unsigned tagl, unsigned tagh) {
  return ((((g.x ^ tagl) | (g.z ^ tagl)) & 0xffff0000u) | (g.y ^ tagh) | (g.w ^ tagh)) == 0u;
}
__device__ __forceinline__ float rs32(float a, float b) {
  auto r = __builtin_amdgcn_permlane32_swap(__float_as_uint(a), __float_as_uint(b), false, false);
  return __uint_as_float(r[0]) + __uint_as_float(r[1]);
}
__device__ __forceinline__ float rs16(float a, float b) {
  auto r = __builtin_amdgcn_permlane16_swap(__float_as_uint(a), __float_as_uint(b), false, false);
  return __uint_as_float(r[0]) + __uint_as_float(r[1]);
}
__device__ __forceinline__ float vmax(float a, float b) {
  float inf = __builtin_inff();
  asm("" : "+s"(inf));
  return __builtin_amdgcn_fmed3f(a, b, inf);
}
template <bool MAX> __device__ __forceinline__ void rows2(float& a, float& b) {
  auto r = __builtin_amdgcn_permlane16_swap(__float_as_uint(a), __float_as_uint(b), false, false);
  float t = MAX ? vmax(__uint_as_float(r[0]), __uint_as_float(r[1])) : __uint_as_float(r[0]) + __uint_as_float(r[1]);
  auto q = __builtin_amdgcn_permlane32_swap(__float_as_uint(t), __float_as_uint(t), false, false);
  t = MAX ? vmax(__uint_as_float(q[0]), __uint_as_float(q[1])) : __uint_as_float(q[0]) + __uint_as_float(q[1]);
  auto u = __builtin_amdgcn_permlane16_swap(__float_as_uint(t), __float_as_uint(t), false, false);
  a = __uint_as_float(u[0]); b = __uint_as_float(u[1]);
}
template <bool MAX> __device__ __forceinline__ float rows4(const float (&v)[4]) {
  auto a = __builtin_amdgcn_permlane32_swap(__float_as_uint(v[0]), __float_as_uint(v[1]), false, false);
  auto b = __builtin_amdgcn_permlane32_swap(__float_as_uint(v[2]), __float_as_uint(v[3]), false, false);
  const float p = MAX ? vmax(__uint_as_float(a[0]), __uint_as_float(a[1])) : __uint_as_float(a[0]) + __uint_as_float(a[1]);
  const float q = MAX ? vmax(__uint_as_float(b[0]), __uint_as_float(b[1])) : __uint_as_float(b[0]) + __uint_as_float(b[1]);
  auto c = __builtin_amdgcn_permlane16_swap(__float_as_uint(p), __float_as_uint(q), false, false);
  return MAX ? vmax(__uint_as_float(c[0]), __uint_as_float(c[1])) : __uint_as_float(c[0]) + __uint_as_float(c[1]);
}
__device__ __forceinline__ void spread4(float x, float (&v)[4]) {
  auto r = __builtin_amdgcn_permlane16_swap(__float_as_uint(x), __float_as_uint(x), false, false);
  auto a = __builtin_amdgcn_permlane32_swap(r[0], r[0], false, false), b = __builtin_amdgcn_permlane32_swap(r[1], r[1], false, false);
  v[0] = __uint_as_float(a[0]); v[1] = __uint_as_float(a[1]); v[2] = __uint_as_float(b[0]); v[3] = __uint_as_float(b[1]);
}
__device__ __forceinline__ unsigned padd(unsigned p, float al, float ah) { return pack2(dot2(p, ONE_LO, al), dot2(p, ONE_HI, ah)); }
__device__ __forceinline__ unsigned pmul(unsigned p, f32x2 m) { return pack2v(unpk(p) * m); }
__device__ __forceinline__ void wait_flags(__amdgpu_buffer_rsrc_t rs, int off, unsigned tagl, unsigned tagh) {
  int sp = 0;
  for (;;) {
    const u32x4 f = ld128(rs, off);
    if (__all(tag_ok(f, tagl, tagh)) || ++sp > SPIN_CAP) break;
    __builtin_amdgcn_s_sleep(2);
  }
  CFENCE();
}
__device__ __forceinline__ void wait_all256(__amdgpu_buffer_rsrc_t rs, int off, unsigned tagl, unsigned tagh) {
  int sp = 0;
  for (;;) {
    const u32x4 f0 = ld128(rs, off), f1 = ld128(rs, off + 1024);
    if (__all(tag_ok(f0, tagl, tagh) & tag_ok(f1, tagl, tagh)) || ++sp > SPIN_CAP) break;
    __builtin_amdgcn_s_sleep(8);
  }
  CFENCE();
}
__device__ __forceinline__ unsigned silu2(unsigned c) {
  const f32x2 f = unpk(c);
  return pack2(f.x * __builtin_amdgcn_rcpf(1.0f + __expf(-f.x)), f.y * __builtin_amdgcn_rcpf(1.0f + __expf(-f.y)));
}
template <int I> struct IC { static constexpr int v = I; };
template <int A, int B, class F> __device__ __forceinline__ void sfor(F&& f) {
  if constexpr (A + 1 == B) f(IC<A>{});
  else if constexpr (A < B) { sfor<A, (A + B) / 2>(f); sfor<(A + B) / 2, B>(f); }
}
template <int N> __device__ __forceinline__ void vmwait() { asm volatile("s_waitcnt vmcnt(%0)" ::"n"(N) : "memory"); }
#define MFMA(c, a, b) asm volatile("v_mfma_f32_16x16x32_bf16 %0, %1, %2, %0" : "+v"(c) : "v"(a), "v"(b))
#define MFMA0(c, a, b) asm volatile("v_mfma_f32_16x16x32_bf16 %0, %1, %2, 0" : "=&v"(c) : "v"(a), "v"(b))
#define LDX(d, voff, soff) asm volatile("buffer_load_dwordx4 %0, %1, %2, %3 offen sc1" : "=&v"(d) : "v"(voff), "s"(rs), "s"(soff) : "memory")
#define LDXO(d, voff, soff, o) asm volatile("buffer_load_dwordx4 %0, %1, %2, %3 offen offset:%4 sc1" : "=&v"(d) : "v"(voff), "s"(rs), "s"(soff), "n"(o) : "memory")
#define READY(d) asm volatile("" : "+v"(d))
#define LDXI(d, r, voff, soff) asm volatile("buffer_load_dwordx4 %0, %1, %2, %3 offen" : "=&v"(d) : "v"(voff), "s"(r), "s"(soff) : "memory")
#define LDXW(d, r, voff, soff) asm volatile("buffer_load_dword %0, %1, %2, %3 offen" : "=&v"(d) : "v"(voff), "s"(r), "s"(soff) : "memory")
#define LDXH(d, r, voff, soff) asm volatile("buffer_load_ushort %0, %1, %2, %3 offen" : "=&v"(d) : "v"(voff), "s"(r), "s"(soff) : "memory")
template <int LOFF, int OFF> __device__ __forceinline__ void dmai(__amdgpu_buffer_rsrc_t r, int voff, int soff, unsigned lds0) {
  asm volatile("s_add_u32 m0, %3, %4\n\ts_nop 0\n\tbuffer_load_dwordx4 %0, %1, %2 offen offset:%5 lds"
               :: "v"(voff), "s"(r), "s"(soff), "s"(lds0), "n"(LOFF - OFF), "n"(OFF) : "m0", "scc", "memory");
}
template <int LOFF, int OFF> __device__ __forceinline__ void dmas(__amdgpu_buffer_rsrc_t r, int voff, int soff, unsigned lds0) {
  asm volatile("s_add_u32 m0, %3, %4\n\ts_nop 0\n\tbuffer_load_dwordx4 %0, %1, %2 offen offset:%5 sc1 lds"
               :: "v"(voff), "s"(r), "s"(soff), "s"(lds0), "n"(LOFF - OFF), "n"(OFF) : "m0", "scc", "memory");
}
template <int SL = 2> __device__ __forceinline__ void wait_flag8(__amdgpu_buffer_rsrc_t rs, int off, u32x2 want) {
  unsigned n;
  u32x2 f;
  asm volatile("s_mov_b32 %1, %5\n"
               "1:\n\tbuffer_load_dwordx2 %0, %2, %3, 0 offen sc1\n\ts_waitcnt vmcnt(0)\n\tv_cmp_ne_u64 vcc, %4, %0\n\ts_cbranch_vccz 2f\n\t"
               "s_sleep %6\n\ts_sub_u32 %1, %1, 1\n\ts_cbranch_scc0 1b\n2:"
               : "=&v"(f), "=&s"(n) : "v"(off), "s"(rs), "s"(want), "n"(SPIN_CAP), "n"(SL) : "vcc", "scc", "memory");
}
#define CODE_BYTES 44800
#define NS1 3
#define GT 150
#define WT 0
#define XDL 8
#define WRD 4
#define U0 8
#define UR 3
#define SP 12
#define XT 6
#define XW 8
#define LM 2
#define HOLD 0
#define HP 0
#define FM 0
#define WDL 18
#define WO2 8
#define UD 1
#define NOLATE 0
#define NOFILL 0
#define NOTOUCH 0
#define HLE 8
#define HP2 0
#define WO3 0
#define FLP 2
#define FAI 7
#define PLX (slot & 1)
#define PTX 2
#define TW2 6
#define WA 0
#define WB 12
#define LP 0
#define XL 12
#define XG 0
#define TS 0
#define TI 3
#define NA 25
#define NB1 0
#define BD 0
#define LG 1
#define CBG 1
#define NOWO 0
#define HALFW 0
#define XT1 3
#define LPR 0
#define WP 0
#define NOWOL 0
#define NOXH 0
#define QW 16
#define OG 2
#define ODL 20
#define SDR 1
#define SDL 8
#define G1N 0
#define G2N 6
#define KG 0
#define RW 7
#define PDL 8
#define PW 7
#define SRS 2
#define GAUX 16
#define SAUX 16
#define TK 6
#define T2G C_OI
#define X1L 1
#define XP 2
#define HE 1
#define WWG C_KD
#define AO 0
#define XMG 1
#define OFS 8
#define OFW OFS
#define OSL 2
#define WSL 2
#define TD 0
#define DWF 0
#define TLBP 0
#define HSG 128
constexpr int xslot(int pair) { return pair == 0 || pair == 9 ? 3 : pair < 4 ? pair - 1 : pair; }
constexpr int uissued(int s) { const int n = U0 + UR * (s + 1) / UD; return n > 40 ? 40 : n; }
constexpr int ulast() { int s = 0; while (uissued(s) < 40) ++s; return s; }
constexpr int gm(int g) { return g < 8 * SP ? g & 7 : ((g - 8 * SP) / (20 - SP) + 7) & 7; }
constexpr int gs(int g) { return g < 8 * SP ? g >> 3 : SP + (g - 8 * SP) % (20 - SP); }
template <int J> __device__ __forceinline__ float mulb(float a, float b) {
  float r = dppf<0x150 + J>(a) * b;
  asm("" : "+v"(r));
  return r;
}
__device__ __forceinline__ float fma1(float a, float b, float c) {
  float r = __builtin_fmaf(a, b, c);
  asm("" : "+v"(r));
  return r;
}
__device__ __forceinline__ void until(unsigned t) {
  while ((int)((unsigned)wall_clock64() - t) < 0) __builtin_amdgcn_s_sleep(1);
}
__device__ __forceinline__ bool none(unsigned v) {
  asm("" : "+v"(v));
  return __builtin_amdgcn_ballot_w64(v != 0u) == 0ull;
}
__device__ __forceinline__ unsigned off2(u32x4 g, unsigned tag) {
  unsigned r = (g.y ^ tag) | (g.w ^ tag);
  asm("" : "+v"(r));
  return r;
}
__device__ __forceinline__ float add1(float a, float b) {
  float r = a + b;
  asm("" : "+v"(r));
  return r;
}
__device__ __forceinline__ u32x4 keys(__amdgpu_buffer_rsrc_t rs, int rep, int l) {
  if (KS == 8) return ld128(rs, WS_KEY + rep * 1024 + l * 16);
  const u32x2 a = ld64(rs, WS_KEY + (rep * 128 + l) * KS), c = ld64(rs, WS_KEY + (rep * 128 + 64 + l) * KS);
  return (u32x4){a.x, a.y, c.x, c.y};
}
template <class T> __device__ __forceinline__ void pin2(T& a, T& b) { asm volatile("" : "+v"(a), "+v"(b)); }
__device__ __forceinline__ int opq(int v) { asm volatile("" : "+s"(v)); return v; }
__device__ __forceinline__ void ready(u32x4& d) { READY(d); }
__device__ __forceinline__ void ldxi(u32x4& d, __amdgpu_buffer_rsrc_t r, int voff, int soff) { LDXI(d, r, voff, soff); }
#define ALL4 0x01010101u
__device__ __forceinline__ void lds_set(char* lds, int idx, int byte) { *((volatile LAS unsigned char*)(lds + L_CNT) + idx * 4 + byte) = 1; }
__device__ __forceinline__ unsigned lds_cnt(char* lds, int idx) { return *((volatile LAS unsigned*)(lds + L_CNT) + idx); }
__device__ __forceinline__ void lds_wait(char* lds, int idx, unsigned m) {
  unsigned v, c, n;
  asm volatile("s_mov_b32 %2, %5\n"
               "1:\n\tds_read_b32 %0, %3\n\ts_waitcnt lgkmcnt(0)\n\tv_readfirstlane_b32 %1, %0\n\ts_andn2_b32 %1, %4, %1\n\ts_cbranch_scc0 2f\n\t"
               "s_sleep 1\n\ts_sub_u32 %2, %2, 1\n\ts_cbranch_scc0 1b\n2:"
               : "=&v"(v), "=&s"(c), "=&s"(n) : "v"((unsigned)(size_t)(LAS char*)lds + L_CNT + idx * 4), "s"(m), "n"(SPIN_CAP) : "scc", "memory");
}
__device__ __forceinline__ void lds_wait2(char* lds, int idx, unsigned m0, unsigned m1) {
  unsigned v0, v1, c0, c1, n;
  asm volatile("s_mov_b32 %4, %8\n"
               "1:\n\tds_read_b32 %0, %5\n\tds_read_b32 %1, %5 offset:4\n\ts_waitcnt lgkmcnt(0)\n\tv_readfirstlane_b32 %2, %0\n\tv_readfirstlane_b32 %3, %1\n\t"
               "s_andn2_b32 %2, %6, %2\n\ts_andn2_b32 %3, %7, %3\n\ts_or_b32 %2, %2, %3\n\ts_cbranch_scc0 2f\n\t"
               "s_sleep 1\n\ts_sub_u32 %4, %4, 1\n\ts_cbranch_scc0 1b\n2:"
               : "=&v"(v0), "=&v"(v1), "=&s"(c0), "=&s"(c1), "=&s"(n) : "v"((unsigned)(size_t)(LAS char*)lds + L_CNT + idx * 4), "s"(m0), "s"(m1), "n"(SPIN_CAP) : "scc", "memory");
}
__device__ __forceinline__ float sum8(float v) {
  v += dppf<0xB1>(v); v += dppf<0x4E>(v);
  return v + dppf<0x141>(v);
}
__device__ __forceinline__ unsigned normalize(unsigned p, float mb, float isd) {
  return pack2v(rbf2(unpk(p) - (f32x2){mb, mb}) * (f32x2){isd, isd});
}

__global__ __launch_bounds__(512, 2) void k_all(const u16*, const u16*, const u16*, const u16*, const u16*, const u16*,
                                                const u16*, const u16*, const u16*, char*, u16*) {
  __shared__ __attribute__((aligned(256))) char lds[163840];
  typedef __attribute__((ext_vector_type(8))) u64 u64x8;
  typedef __attribute__((ext_vector_type(2))) u64 u64x2;
  u64x8 ka0;
  u64x2 ka1;
  u64 ka2;
  asm volatile("s_load_dwordx16 %0, %3, 0\n\ts_load_dwordx4 %1, %3, 0x40\n\ts_load_dwordx2 %2, %3, 0x50"
               : "=&s"(ka0), "=&s"(ka1), "=&s"(ka2) : "s"(__builtin_amdgcn_kernarg_segment_ptr()));
  int t = threadIdx.x, l = t & 63;
  const int b = blockIdx.x, xcd = b & 7, slot = b >> 3;
  unsigned pfc;
  {
    const __amdgpu_buffer_rsrc_t rcode = __builtin_amdgcn_make_buffer_rsrc((void*)__builtin_amdgcn_s_getpc(), 0, CODE_BYTES, 0x00020000);
    LDXW(pfc, rcode, t * 4096, slot * 128);
  }
  const int w = __builtin_amdgcn_readfirstlane(t >> 6);
  int g4 = l >> 4, i16 = l & 15;
  const int mk = (slot & 1) * 4 + (xcd & 3), ml = (slot >> 1) + 10 - (xcd >> 2) * 10;
  const bool hasm = w == 3 && slot < 20;
  const bool prod = slot < 30;
  const int rb = (xcd >> 1) * 4 + (slot >> 3), m0 = slot & 7, hp = (xcd & 1) * 8 + m0;
  const int rg = ((xcd + 4) & 7) * 4 + (slot >> 3), cg = slot & 7;
  const int rc = rb * 16 + m0 * 2 + (xcd & 1);
  const unsigned lbase = (unsigned)(size_t)(LAS char*)lds;
  const int o0 = 2 * w + min(w, 2);
  const int Fp = (255 - b) * 4 + w, kc = Fp / 10, ln = Fp - kc * 10, hq5 = kc / 5;
  int xo = (l >> 3) * 1280 + (((l & 7) ^ (l >> 3)) * 16);
  int mo = (l >> 3) * 15360 + (l & 7) * 16, co2 = l * 4, wsrc0 = (l >> 3) * 3840 + (l & 7) * 16;
  int osrc0 = (((l >> 3) & 3) + 20 * (l >> 5)) * 1280 + (l & 7) * 16;
  int mso = mk * 80 * 7680 + ml * 128, cso = mk * 160;
  *(unsigned*)(lds + L_CNT + l * 4) = 0u;
  asm volatile("" : "+v"(xo), "+v"(mo), "+v"(co2), "+v"(wsrc0), "+v"(osrc0));
  asm volatile("" : "+s"(mso), "+s"(cso));
  STAMP_INIT();
  SBAR();
  asm volatile("s_waitcnt lgkmcnt(0)" : "+s"(ka0), "+s"(ka1), "+s"(ka2));
  const u16 *x = (const u16*)ka0[0], *wqkv = (const u16*)ka0[1], *wo = (const u16*)ka0[2], *wr = (const u16*)ka0[3];
  const u16 *wg = (const u16*)ka0[4], *wu = (const u16*)ka0[5], *wd = (const u16*)ka0[6], *cvec = (const u16*)ka0[7];
  const u16* wmod = (const u16*)ka1[0];
  char* ws = (char*)ka1[1];
  u16* out = (u16*)ka2;
  const __amdgpu_buffer_rsrc_t rs = __builtin_amdgcn_make_buffer_rsrc(ws, 0, WS_END, 0x00020000);
  const __amdgpu_buffer_rsrc_t rx = __builtin_amdgcn_make_buffer_rsrc((void*)x, 0, S_LEN * DM * 2, 0x00020000);

  const int lu = Fp - 4, ll = lu / 20, lk = lu - ll * 20;
  u32x4 wm[10], rp, cl;
  unsigned cv;
  u32x2 epv;
  const __amdgpu_buffer_rsrc_t rm = __builtin_amdgcn_make_buffer_rsrc((void*)wmod, 0, DM * 7680, 0x00020000);
  const __amdgpu_buffer_rsrc_t rcv = __builtin_amdgcn_make_buffer_rsrc((void*)cvec, 0, DM * 2, 0x00020000);
  if (hasm) {
    LDXW(cv, rcv, co2, cso);
#pragma unroll
    for (int i = 0; i < 10; ++i) LDXI(wm[i], rm, mo, mso + (16 * (i >> 1) + (i & 1)) * 7680);
  }
  asm volatile("buffer_load_dwordx2 %0, off, %1, 0 sc1" : "=&v"(epv) : "s"(rs) : "memory");
  u64 t0;
  asm volatile("s_memrealtime %0" : "=s"(t0));
  SBAR();
  __builtin_amdgcn_s_barrier();
  SBAR();
  if (opq(w) < 4) {
    if (WRD) __builtin_amdgcn_s_sleep(WRD);
    const int s1 = (xcd * 80 + o0 * 8) * 3840 + slot * 128, s2 = (hq5 * 40 + (kc - hq5 * 5) * 4) * 1280 + ln * 128;
    const unsigned d1 = lbase + o0 * 1024;
    const __amdgpu_buffer_rsrc_t rw = __builtin_amdgcn_make_buffer_rsrc((void*)wqkv, 0, prod ? DM * 3840 : 0, 0x00020000);
    dmai<L_STG, 0>(rw, wsrc0, s1, d1);
    dmai<L_STG + 1024, 0>(rw, wsrc0, s1 + 8 * 3840, d1);
    if (w < 2) dmai<L_STG + 2048, 0>(rw, wsrc0, s1 + 16 * 3840, d1);
    const __amdgpu_buffer_rsrc_t r1 = __builtin_amdgcn_make_buffer_rsrc((void*)wo, 0, Fp < 800 ? DM * 1280 : 0, 0x00020000);
    LDXI(rp, r1, osrc0, s2);
  } else {
    LDXI(cl, rcv, g4 * 16, lu < 800 ? lk * 64 : 0);
    if (XDL) __builtin_amdgcn_s_sleep(XDL);
    const int s1 = (rb * 128 + (2 * m0 + (w & 1)) * 8) * 1280;
    const unsigned d1 = lbase + (2 * m0 + (w & 1)) * 1024;
    if (w < 6) {
      sfor<0, 5>([&](auto I) { constexpr int pr = decltype(I)::v; dmai<L_X + xslot(pr) * 16384, pr * 128>(rx, xo, s1, d1); });
    } else {
      sfor<5, 9>([&](auto I) { constexpr int pr = decltype(I)::v; dmai<L_X + xslot(pr) * 16384, pr * 128>(rx, xo, s1, d1); });
      dmai<L_X9, 9 * 128>(rx, xo, s1, lbase + (w & 1) * 1024);
    }
  }
  asm volatile("s_waitcnt lgkmcnt(0)" : "+s"(t0));
  STAMP(30);
  unsigned tagl, tagh, tag32, ep0, ep1;
  u32x2 flagv;
#define MAKE_TAG(n) do { \
    asm volatile("s_waitcnt vmcnt(%4)\n\tv_readfirstlane_b32 %0, %2\n\tv_readfirstlane_b32 %1, %3" : "=s"(ep0), "=s"(ep1) : "v"(epv.x), "v"(epv.y), "n"(n) : "memory"); \
    const u64 tag = ((((u64)ep1 << 32) | ep0) + 1ull) & 0xffffffffffffull; \
    tagl = (unsigned)(tag << 16); tagh = (unsigned)(tag >> 16); tag32 = (unsigned)tag; \
    flagv = (u32x2){tagl | 1u, tagh}; \
    asm volatile("" : "+s"(tagl), "+s"(tagh), "+s"(tag32), "+s"(flagv)); \
  } while (0)
  if (hasm) {
    int bpa = (l >> 3) * 4;
    int mdst = (l & 8) ? OOB : WS_MODP + mk * 30720 + (ml * 64 + 8 * (l & 7) + 4 * (l >> 5) + 2 * ((l >> 4) & 1)) * 8;
    float acc[8];
#pragma unroll
    for (int jj = 0; jj < 8; ++jj) acc[jj] = 0.f;
    asm volatile("" : "+v"(bpa), "+v"(mdst), "+v"(acc[0]), "+v"(acc[1]), "+v"(acc[2]), "+v"(acc[3]), "+v"(acc[4]), "+v"(acc[5]), "+v"(acc[6]), "+v"(acc[7]));
    vmwait<14>();
    READY(cv);
    const unsigned sp = silu2(cv);
    unsigned sv[5];
#pragma unroll
    for (int i = 0; i < 5; ++i) sv[i] = __builtin_amdgcn_ds_bpermute(bpa + 32 * i, sp);
    SBAR();
    sfor<0, 5>([&](auto I) {
      constexpr int i = decltype(I)::v;
      vmwait<12 - 2 * i>();
      ready(wm[2 * i]); ready(wm[2 * i + 1]);
#pragma unroll
      for (int jj = 0; jj < 4; ++jj) {
        acc[2 * jj] = dot2(__builtin_amdgcn_perm(wm[2 * i + 1][jj], wm[2 * i][jj], 0x05040100u), sv[i], acc[2 * jj]);
        acc[2 * jj + 1] = dot2(__builtin_amdgcn_perm(wm[2 * i + 1][jj], wm[2 * i][jj], 0x07060302u), sv[i], acc[2 * jj + 1]);
      }
      SBAR();
    });
    float v0 = rs32(acc[0], acc[4]), v1 = rs32(acc[1], acc[5]), v2 = rs32(acc[2], acc[6]), v3 = rs32(acc[3], acc[7]);
    v0 = rs16(v0, v2); v1 = rs16(v1, v3);
    v0 += dppf<0x128>(v0); v1 += dppf<0x128>(v1);
    MAKE_TAG(3);
    ST128(((u32x4){__float_as_uint(v0), tag32, __float_as_uint(v1), tag32}), mdst);
  }
  STAMP(3);
  const int Ggu = (slot >> 2) * 8 + xcd;
  const bool is_gu = slot < 16 && Ggu < 27;
  const int fb = Ggu * 4 + (slot & 3);
  const bool is_dn = b >= 128 && b < 168;
  const int dnb = b - 128;
  const int m2 = w & 3, kh = w >> 2;
  const __amdgpu_buffer_rsrc_t ro = __builtin_amdgcn_make_buffer_rsrc((void*)out, 0, S_LEN * DM * 2, 0x00020000);
  u32x4 dw[4];
#define DW_LOAD() do { \
    const __amdgpu_buffer_rsrc_t rd = __builtin_amdgcn_make_buffer_rsrc((void*)wd, 0, FF * 1280, 0x00020000); \
    const int o = (is_gu && w >= 3) ? (fb * 16 + g4 * 4) * 1280 + (w - 3) * 256 + i16 * 16 : OOB; \
    _Pragma("unroll") for (int i = 0; i < 4; ++i) dw[i] = __builtin_amdgcn_raw_buffer_load_b128(rd, o, i * 1280, 0); \
    SBAR(); \
  } while (0)
  bf16x8 af[5];
  unsigned dp[2][8];
  int hbo, mro, ydst, bdst;
  unsigned x1[10];
#define MLP_PREP() do { \
    hbo = L_H2 + (w & 3) * 64 + g4 * 16; mro = i16 == 0 ? L_MRED + w * 64 + g4 * 16 : L_MRED + 1024; \
    ydst = w >= 3 ? WS_YP + fb * 5120 + (w - 3) * 1024 + l * 16 : OOB; \
    lds_wait(lds, C_GL, ALL4); \
    const char* stg = lds + L_GU + (kh * 20 + m2) * 1024 + g4 * 256 + i16 * 8; \
    _Pragma("unroll") for (int i = 0; i < 5; ++i) { \
      const s16x4 lo = trread(stg + i * 4096), hi = trread(stg + i * 4096 + 128); \
      af[i] = __builtin_shufflevector(lo, hi, 0, 1, 2, 3, 4, 5, 6, 7); \
      asm volatile("" : "+v"(af[i])); \
    } \
    _Pragma("unroll") for (int p = 0; p < 2; ++p) \
      _Pragma("unroll") for (int k = 0; k < 4; ++k) { \
        dp[p][2 * k] = __builtin_amdgcn_perm(dw[2 * p + 1][k], dw[2 * p][k], 0x05040100u); \
        dp[p][2 * k + 1] = __builtin_amdgcn_perm(dw[2 * p + 1][k], dw[2 * p][k], 0x07060302u); \
        asm volatile("" : "+v"(dp[p][2 * k]), "+v"(dp[p][2 * k + 1])); \
      } \
    asm volatile("" : "+v"(hbo), "+v"(mro), "+v"(ydst)); \
  } while (0)
  if (opq(w) < 4) {
    int rdst, cdst, goff, fh, fpo, odsto, tra = L_STG + o0 * 1024 + (i16 >> 2) * 128 + g4 * 32 + (i16 & 3) * 8;
    int twa = L_STG + o0 * 1024 + l * 16, mra = L_MRED + l * 8;
    int tro = ln < 8 ? L_STG + o0 * 1024 + (i16 >> 2) * 128 + (g4 >> 1) * 64 + (i16 & 3) * 16 + (g4 & 1) * 8 : tra;
    {
      const int co = slot * 8 + (l >> 3), part = co >= 160 ? 2 : co >= 80 ? 1 : 0, r = co - part * 80, hh = (r * 205) >> 10;
      const int pos = (part == 0 ? 80 : part * 40 - 40) + (r - hh * 5) * 8 + (l & 7);
      rdst = (prod && (!HALFW || (l & 8))) ? hh * 163840 + (pos >> 4) * 1024 + (pos & 15) * 16 : OOB;
      cdst = prod ? WS_CB + (xcd * 16 + hh) * 2048 + pos * 16 : OOB;
      const int np = w < 2 ? 11 : 7;
      goff = WS_MODP + g4 * 30720 + (xcd * 80 + o0 * 8 + 2 * (i16 < np ? i16 : np)) * 8;
      fh = l == 0 ? (prod ? WS_FLAGW + b * 16 : OOB) : (l == 1 ? WS_FLAGP + b * 8 : OOB);
      const int cls = (w == 0 ? 1 : w == 3 ? 0 : (l & 16) ? 2 : (w == 1 ? 1 : 0));
      fpo = WS_FLAGW + ((cls * 80 + 5 * hp + ((l >> 3) & 1) * 4) >> 3) * 128 + (l & 7) * 16;
      const int fo = ln < 8 ? ln * 102400 + g4 * 1024 : (4 * (ln - 8) + g4) * 102400 + 4096;
      odsto = (Fp < 800 && !NOWO) ? WS_WOT + fo + (kc >> 2) * 5120 + (kc & 3) * 256 + i16 * 16 : OOB;
    }
    unsigned cnt = l == 0 ? 1u : 0u, ca = lbase + L_CNT + ((C_HD + l) & 63) * 4;
    asm volatile("" : "+v"(rdst), "+v"(cdst), "+v"(goff), "+v"(fh), "+v"(fpo), "+v"(odsto), "+v"(tra), "+v"(tro), "+v"(twa), "+v"(mra), "+v"(cnt), "+v"(ca));
    int wso[3];
#pragma unroll
    for (int r = 0; r < 3; ++r) { const int ko = xcd * 10 + o0 + r; wso[r] = opq(WS_WT + (ko >> 2) * 8192 + (ko & 3) * 256); }
    STAMP(2);
    f32x4 mbias[2];
    int odst[2], odst4[2], kbo[2], vbo[2], qa[2][2][4], fo;
    unsigned me = (i16 & 1) ? 0u : 0xffffffffu;
    {
      const int kc1 = 5 * hp + g4, kc4 = 5 * hp + 4;
      fo = l < 2 ? WS_FLAGO + (rb * 8 + ((w + 1 + 4 * l) & 7)) * 128 + hp * 8 : OOB;
      if (OFS > 8) fo = (l < 32 && (l & 15) < OFW / 8) ? WS_FO2 + ((rb * 8 + ((w + 1 + 4 * g4) & 7)) * 16 + hp) * OFS + i16 * 8 : OOB;
#pragma unroll
      for (int jj = 0; jj < 4; ++jj) {
        const int kk = 4 * g4 + jj;
        mbias[0][jj] = (kk >= (i16 >> 1) + 1 && kk <= (i16 >> 1) + 8) ? 0.f : -1.0e30f;
        mbias[1][jj] = (rb == 0 && w == 3 && g4 < 2) ? -1.0e30f : mbias[0][jj];
      }
#pragma unroll
      for (int a = 0; a < 2; ++a) {
        const int to = L_KV + ((w + 1 + 4 * a) & 7) * 16 * KVS;
        const int mo2 = WS_O + (rb * 8 + ((w + 1 + 4 * a) & 7)) * 20480;
        odst[a] = mo2 + (kc1 >> 2) * 1024 + ((kc1 & 3) * 16 + i16) * 16;
        odst4[a] = l < 16 ? mo2 + (kc4 >> 2) * 1024 + ((kc4 & 3) * 16 + i16) * 16 : OOB;
        kbo[a] = to + 2 * i16 * KVS + g4 * 8;
        vbo[a] = to + (8 * g4 + 2 * ((l >> 2) & 3)) * KVS + 80 + (l & 3) * 8;
        const int qo = to + (16 + i16) * KVS + 160 + g4 * 8, zo = L_CNT + 128 + i16 * 8;
#pragma unroll
        for (int par = 0; par < 2; ++par) {
          const bool pm = (i16 & 1) == par;
          qa[a][par][0] = pm ? qo : zo;
          qa[a][par][1] = (pm && g4 == 0) ? qo + 32 : zo;
          qa[a][par][2] = (pm && g4 != 0) ? qo + 32 : zo;
          qa[a][par][3] = (pm && g4 < 2) ? qo + 64 : zo;
          asm volatile("" : "+v"(qa[a][par][0]), "+v"(qa[a][par][1]), "+v"(qa[a][par][2]), "+v"(qa[a][par][3]));
        }
        asm volatile("" : "+v"(kbo[a]), "+v"(vbo[a]), "+v"(mbias[a]), "+v"(odst[a]), "+v"(odst4[a]));
      }
      asm volatile("" : "+v"(fo), "+v"(me));
    }
    const int wsrc = WS_WT + hp * 163840 + w * 2048;
    int uo0 = l * 16, uo1 = (w < 3 || i16 < 8) ? l * 16 + 1024 : OOB;
    int xb0 = L_X + (i16 >> 3) * 1024 + (i16 & 7) * 128 + ((g4 ^ (i16 & 7)) * 16), xb1 = xb0 ^ 64;
    int xc0 = xb0 + 65536, xc1 = xb1 + 65536, xd0 = xb0 + 131072, xd1 = xb1 + 131072;
    int kvo0 = L_KV + (16 + i16) * KVS + w * 64 + g4 * 8;
    int kvo1 = kvo0 + 32 - ((w == 3 && g4 == 3) ? 8 : 0);
    int hdo0 = w < 3 ? WS_HALO + (rb * 16 + hp) * 5120 + w * 2048 + l * 16 : OOB, hdo1 = w < 2 ? hdo0 + 1024 : OOB;
    int cso = L_CS + w * 128 + g4 * 16, sto = L_CS + 1024 + i16 * 8;
    asm volatile("" : "+v"(uo0), "+v"(uo1), "+v"(xb0), "+v"(xb1), "+v"(xc0), "+v"(xc1), "+v"(xd0), "+v"(xd1));
    asm volatile("" : "+v"(kvo0), "+v"(kvo1), "+v"(hdo0), "+v"(hdo1), "+v"(cso), "+v"(sto));
    STAMP(31);
    until((unsigned)t0 + GT);
    {
      u32x4 g[2], h[2];
      LDX(g[0], goff, 5120); LDX(g[1], goff, 122880 + 5120);
      LDX(h[0], goff, 0); LDX(h[1], goff, 122880);
    if (hasm) vmwait<4>(); else MAKE_TAG(4);
    asm volatile("" ::"v"(pfc));
    READY(rp);
    STAMP(4);
    f32x2 wf[3][4];
    u32x4 wraw[3];
#pragma unroll
    for (int r = 0; r < 3; ++r) {
      const u32x2 lo = __builtin_bit_cast(u32x2, trread(lds + tra + r * 1024)), hi = __builtin_bit_cast(u32x2, trread(lds + tra + r * 1024 + 512));
      wraw[r] = (u32x4){lo.x, lo.y, hi.x, hi.y};
    }
    CFENCE();
    *(u32x4*)(lds + twa) = rp;
    CFENCE();
    {
      const u32x2 lo = __builtin_bit_cast(u32x2, trread(lds + tro)), hi = __builtin_bit_cast(u32x2, trread(lds + tro + 512));
      ST128(((u32x4){lo.x, lo.y, hi.x, hi.y}), odsto);
    }
#pragma unroll
    for (int r = 0; r < 3; ++r)
#pragma unroll
      for (int jj = 0; jj < 4; ++jj) { wf[r][jj] = unpk(wraw[r][jj]); asm volatile("" : "+v"(wf[r][jj])); }
      STAMP(5);
      vmwait<3>();
      READY(g[0]); READY(g[1]);
#define GOOD2(g) none(off2(g[0], tag32) | off2(g[1], tag32))
#define RETRY2(g, so) do { \
        if (__builtin_expect(!GOOD2(g), 0)) { \
          int sp_ = 0; \
          do { \
            __builtin_amdgcn_s_sleep(2); \
            g[0] = ld128(rs, goff + (so)); g[1] = ld128(rs, goff + 122880 + (so)); \
          } while (!GOOD2(g) && ++sp_ < SPIN_CAP); \
        } \
      } while (0)
#define GOOD4(g) none(off2(g[0], tag32) | off2(g[1], tag32) | off2(g[2], tag32) | off2(g[3], tag32))
#define RETRY4(g, vo, st, so) do { \
        if (__builtin_expect(!GOOD4(g), 0)) { \
          int sp_ = 0; \
          do { \
            __builtin_amdgcn_s_sleep(2); \
            _Pragma("unroll") for (int q = 0; q < 4; ++q) g[q] = ld128(rs, (vo) + q * (st) + (so)); \
          } while (!GOOD4(g) && ++sp_ < SPIN_CAP); \
        } \
      } while (0)
#define SUM4(g, c) add1(add1(__uint_as_float(g[0].c), __uint_as_float(g[1].c)), add1(__uint_as_float(g[2].c), __uint_as_float(g[3].c)))
      RETRY2(g, 5120);
      STAMP(6);
      float a = __uint_as_float(g[0].x) + __uint_as_float(g[1].x), c = __uint_as_float(g[0].z) + __uint_as_float(g[1].z);
      rows2<false>(a, c);
      const f32x2 c1 = rbf2(rbf2((f32x2){a, c}) + (f32x2){1.0f, 1.0f});
      u32x4 wp[3];
      sfor<0, 3>([&](auto R) {
        constexpr int r = decltype(R)::v;
        if (r < 2 || w < 2) {
          sfor<0, 4>([&](auto J) {
            constexpr int jj = decltype(J)::v;
            wp[r][jj] = pack2(mulb<4 * r + jj>(c1.x, wf[r][jj].x), mulb<4 * r + jj>(c1.y, wf[r][jj].y));
          });
          __builtin_amdgcn_raw_buffer_store_b128(wp[r], rs, rdst, wso[r], SC1);
        }
      });
      SBAR();
      STAMP(1);
      if (w < 2) vmwait<4>(); else vmwait<3>();
      READY(h[0]); READY(h[1]);
      RETRY2(h, 0);
      a = __uint_as_float(h[0].x) + __uint_as_float(h[1].x); c = __uint_as_float(h[0].z) + __uint_as_float(h[1].z);
      rows2<false>(a, c);
      const unsigned sa2 = pack2(a, c);
      float cs = 0.f, bi = 0.f;
      sfor<0, 3>([&](auto R) {
        constexpr int r = decltype(R)::v;
        if (r < 2 || w < 2) {
          sfor<0, 4>([&](auto J) {
            constexpr int jj = decltype(J)::v;
            cs = dot2(wp[r][jj], 0x3f803f80u, cs);
            bi = dot2(wraw[r][jj], dppu<0x150 + 4 * r + jj>(sa2), bi);
          });
        }
      });
      *(f32x2*)(lds + mra + w * 512) = (f32x2){cs, bi};
    }
    asm volatile("s_waitcnt vmcnt(0)\n\tds_add_rtn_u32 %0, %1, %0\n\ts_waitcnt lgkmcnt(0)" : "+v"(cnt) : "v"(ca) : "memory");
    if (__builtin_amdgcn_readfirstlane(cnt) == 3u) {
      ST64(flagv, fh);
      const f32x2 v = (*(const f32x2*)(lds + mra) + *(const f32x2*)(lds + mra + 512)) + (*(const f32x2*)(lds + mra + 1024) + *(const f32x2*)(lds + mra + 1536));
      ST128(((u32x4){__float_as_uint(v.x), tag32, __float_as_uint(v.y), tag32}), cdst);
    }
    STAMP(28);

    if (PW & 1) ST64(((u32x2){0u, ~tag32}), WS_ST2 + ((rg * 4 + w) * 8 + cg) * 512 + l * 8);
    if (PW & 2) ST64(((u32x2){0u, ~tag32}), l == 0 ? WS_KEY + (cg * 128 + rg * 4 + w) * KS : OOB);
    if (PW & 4) ST128(((u32x4){0u, ~tag32, 0u, ~tag32}), (is_gu && w == 3) ? WS_YP + fb * 5120 + l * 16 : OOB);
    if (PW & 8) ST64(((u32x2){0u, ~tagh}), fo);
    STAMP(29);
    if (WDL) { lds_wait(lds, C_HD, 4u); __builtin_amdgcn_s_sleep(WDL); }
    wait_flag8<WSL>(rs, fpo, flagv);
    STAMP(16);
    u32x4 u[40];
    f32x4 acc[8][2];
    auto uld = [&](auto K) __attribute__((always_inline)) {
      constexpr int k = decltype(K)::v;
      u[k] = LD128S((k & 1) ? uo1 : uo0, wsrc + (k >> 1) * 8192);
    };
    sfor<0, U0>(uld);
    SBAR();
    lds_set(lds, C_UI, w);
    lds_wait(lds, C_XA, ALL4);
    constexpr int NB = 20 - SP;
    static_assert(SP >= 10 && NB >= 8, "");
    auto xfrag = [&](int g) __attribute__((always_inline)) {
      const int m = gm(g), s = gs(g), sl = xslot(s >> 1);
      const int base = sl < 4 ? ((s & 1) ? xb1 : xb0) : sl < 8 ? ((s & 1) ? xc1 : xc0) : ((s & 1) ? xd1 : xd0);
      return *(const bf16x8*)(lds + base + (sl & 3) * 16384 + m * 2048);
    };
    constexpr int XR = XW + 2;
    static_assert(XW % 2 == 0, "");
    bf16x8 xq[XR];
#pragma unroll
    for (int g = 0; g < XW; ++g) { xq[g ^ 1] = xfrag(g ^ 1); SBAR(); }
    SBAR();
    f32x4 cs[2], bi[2], et[2];
    f32x2 st[2];
    u32x2 pk;
    unsigned rdy, rdys;
    auto epi = [&](auto M, auto K) __attribute__((always_inline)) {
      constexpr int m = decltype(M)::v, n = decltype(K)::v / 11, k = decltype(K)::v % 11;
      if constexpr (n < 2) {
        if constexpr (k < 4) et[n][k] = fma1(cs[n][k], st[m & 1].y, bi[n][k]);
        else if constexpr (k < 8) et[n][k - 4] = fma1(acc[m][n][k - 4], st[m & 1].x, et[n][k - 4]);
        else if constexpr (k == 8) pk.x = pack2v(et[n].xy);
        else if constexpr (k == 9) pk.y = pack2v(et[n].zw);
        else {
          if constexpr (m == 7) __builtin_amdgcn_raw_buffer_store_b128(((u32x4){pk.x, tag32, pk.y, tag32}), rs, n ? hdo1 : hdo0, 0, SC1);
          *(u32x2*)(lds + (n ? kvo1 : kvo0) + m * 16 * KVS) = pk;
        }
      }
    };
    constexpr int G1 = 48 - XW, G2 = 80 - XW, G3 = 8 * SP + NB + 3, G4 = 8 * SP + NB - 2 - XW;
    static_assert(G4 >= G2 + 5 && G3 >= G4 + 5, "");
    sfor<0, 160>([&](auto G) {
      constexpr int g = decltype(G)::v, m = gm(g), s = gs(g), e = g < 8 * SP ? -1 : (g - 8 * SP) % NB;
      constexpr bool ep = g >= 8 * SP + NB;
      constexpr int pm = ep ? gm(g - NB) : 0;
      if constexpr (g == G1 || g == G2 || g == G3 || g == G4) {
        if (__builtin_expect(rdys != ALL4, 0)) lds_wait(lds, g == G1 ? C_XB : g == G2 ? C_XC : g == G3 ? C_KV : C_X9, ALL4);
        CFENCE();
      }
      if (s == 0) MFMA0(acc[m][0], __builtin_bit_cast(bf16x8, u[2 * s]), xq[g % XR]);
      else MFMA(acc[m][0], __builtin_bit_cast(bf16x8, u[2 * s]), xq[g % XR]);
      SBAR();
      if constexpr (g + XW < 160) xq[((g + XW) ^ 1) % XR] = xfrag((g + XW) ^ 1);
      if constexpr (ep) epi(IC<pm>{}, IC<3 * e>{});
      SBAR();
      if (s == 0) MFMA0(acc[m][1], __builtin_bit_cast(bf16x8, u[2 * s + 1]), xq[g % XR]);
      else MFMA(acc[m][1], __builtin_bit_cast(bf16x8, u[2 * s + 1]), xq[g % XR]);
      SBAR();
      {
        constexpr int sg = g >> 3, n0 = uissued(sg - 1), n1 = uissued(sg), f = g & 7;
        if constexpr ((f & 1) == 0 && n0 + f / 2 < n1) uld(IC<n0 + f / 2>{});
      }
      if constexpr (ep) { epi(IC<pm>{}, IC<3 * e + 1>{}); epi(IC<pm>{}, IC<3 * e + 2>{}); }
      if constexpr (g + 5 == G1 || g + 5 == G2 || g + 5 == G3 || g + 5 == G4) rdy = lds_cnt(lds, g + 5 == G1 ? C_XB : g + 5 == G2 ? C_XC : g + 5 == G3 ? C_KV : C_X9);
      if constexpr (g + 2 == G1 || g + 2 == G2 || g + 2 == G3 || g + 2 == G4) rdys = __builtin_amdgcn_readfirstlane(rdy);
      if constexpr (g >= 8 * SP + 1 && g < 8 * SP + 9 && (g & 1)) {
        constexpr int i = (g - 8 * SP) / 2;
        if constexpr (i < 2) cs[i] = *(const f32x4*)(lds + cso + i * 64); else bi[i - 2] = *(const f32x4*)(lds + cso + 512 + (i - 2) * 64);
      }
      if constexpr (e == NB - 5) st[m & 1] = *(const f32x2*)(lds + sto + m * 128);
      if constexpr (g == 16) lds_set(lds, C_CH, w);
      if constexpr (g == 64) lds_set(lds, C_KV, w);
      if constexpr (g == 8 * ulast() + 7 - HLE) lds_set(lds, C_HL, w);
      if constexpr (g == HSG) lds_set(lds, C_GO, w);
      if constexpr (g == 160 - NB) lds_set(lds, C_K4, w);
      if constexpr (g == 156) rdy = lds_cnt(lds, C_K4);
      if constexpr (g == 159) rdys = __builtin_amdgcn_readfirstlane(rdy);
      SBAR();
    });
    typedef __attribute__((ext_vector_type(6))) unsigned u32x6;
    u32x6 kk[2][2];
    u32x4 qb[2][2][2];
    bf16x8 vf[2][2][2];
    f32x4 sc[2][2];
    float sm[2][2];
    auto operands = [&](int a) __attribute__((always_inline)) {
      const char* kb = lds + kbo[a];
#pragma unroll
      for (int par = 0; par < 2; ++par) {
        const u32x2 k0 = *(const u32x2*)(kb + par * KVS), k1 = *(const u32x2*)(kb + par * KVS + 32), k2 = *(const u32x2*)(kb + par * KVS + 64);
        kk[a][par] = (u32x6){k0.x, k0.y, k1.x, k1.y, k2.x, k2.y};
#pragma unroll
        for (int h = 0; h < 2; ++h) {
          const u32x2 e = *(const u32x2*)(lds + qa[a][par][2 * h]), c = *(const u32x2*)(lds + qa[a][par][2 * h + 1]);
          qb[a][h][par] = (u32x4){e.x, e.y, c.x, c.y};
        }
      }
      const char* vb = lds + vbo[a];
#pragma unroll
      for (int h = 0; h < 2; ++h)
#pragma unroll
        for (int d = 0; d < 2; ++d) {
          const s16x4 lo = trread(vb + h * 40 + d * 32), hi = trread(vb + KVS + h * 40 + d * 32);
          vf[a][h][d] = __builtin_shufflevector(lo, hi, 0, 1, 2, 3, 4, 5, 6, 7);
        }
    };
    auto scores = [&](int a) __attribute__((always_inline)) {
      pin2(kk[a][0], kk[a][1]);
      sc[a][0] = mbias[a]; sc[a][1] = mbias[a];
#pragma unroll
      for (int par = 0; par < 2; ++par) {
        sc[a][0] = __builtin_amdgcn_mfma_f32_16x16x32_bf16(__builtin_bit_cast(bf16x8, __builtin_shufflevector(kk[a][par], kk[a][par], 0, 1, 2, 3)),
                                                           __builtin_bit_cast(bf16x8, qb[a][0][par]), sc[a][0], 0, 0, 0);
        sc[a][1] = __builtin_amdgcn_mfma_f32_16x16x32_bf16(__builtin_bit_cast(bf16x8, __builtin_shufflevector(kk[a][par], kk[a][par], 2, 3, 4, 5)),
                                                           __builtin_bit_cast(bf16x8, qb[a][1][par]), sc[a][1], 0, 0, 0);
      }
    };
    auto soft = [&](int a) __attribute__((always_inline)) {
      float mx[2];
#pragma unroll
      for (int h = 0; h < 2; ++h) mx[h] = vmax(fmaxf(fmaxf(sc[a][h].x, sc[a][h].y), sc[a][h].z), sc[a][h].w);
      rows2<true>(mx[0], mx[1]);
#pragma unroll
      for (int h = 0; h < 2; ++h) {
        const float k2 = 0.22360679774997896f * 1.4426950408889634f, nm = mx[h] * -k2;
#pragma unroll
        for (int k = 0; k < 4; ++k) sc[a][h][k] = __builtin_amdgcn_exp2f(__builtin_fmaf(sc[a][h][k], k2, nm));
        sm[a][h] = (sc[a][h][0] + sc[a][h][1]) + (sc[a][h][2] + sc[a][h][3]);
      }
      rows2<false>(sm[a][0], sm[a][1]);
    };
    auto finish = [&](int a) __attribute__((always_inline)) {
      f32x4 od[2][2];
#pragma unroll
      for (int h = 0; h < 2; ++h) {
        const f32x4 p = sc[a][h] * __builtin_amdgcn_rcpf(sm[a][h]);
        const unsigned p01 = pack2v(p.xy), p23 = pack2v(p.zw);
        const bf16x8 pf = __builtin_bit_cast(bf16x8, ((u32x4){p01 & me, p23 & me, p01 & ~me, p23 & ~me}));
#pragma unroll
        for (int d = 0; d < 2; ++d)
          od[h][d] = __builtin_amdgcn_mfma_f32_16x16x32_bf16(vf[a][h][d], pf, (f32x4){0.f, 0.f, 0.f, 0.f}, 0, 0, 0);
      }
      ST128(((u32x4){pack2v(od[0][0].xy), pack2v(od[0][0].zw), pack2v(od[1][0].xy), pack2v(od[1][0].zw)}), odst[a]);
      ST128(((u32x4){pack2v(od[0][1].xy), pack2v(od[0][1].zw), pack2v(od[1][1].xy), pack2v(od[1][1].zw)}), odst4[a]);
    };
    if (__builtin_expect(rdys != ALL4, 0)) lds_wait(lds, C_K4, ALL4);
    CFENCE();
    operands(0);
    SBAR();
    STAMP(22);
    sfor<0, 22>([&](auto K) { epi(IC<6>{}, K); });
    CFENCE();
    lds_set(lds, C_KD, w);
    SBAR();
    STAMP(23);
    scores(0);
    soft(0);
    SBAR();
    lds_wait2(lds, C_KD, ALL4, (w + 1) >> 2);
    STAMP(7);
    operands(1);
    SBAR();
    if (AO == 0) { finish(0); SBAR(); scores(1); soft(1); }
    else { scores(1); finish(0); soft(1); }
    finish(1);
    SBAR();
    asm volatile("" : "+v"(t));
    l = t & 63; g4 = l >> 4; i16 = l & 15;
    STAMP(25);
    const int mt = rg * 4 + w;
    int xro = (mt * 16 + i16) * 1280 + cg * 128 + g4 * 16, xre = (mt * 16 + i16) * 1280 + 1024 + cg * 32 + g4 * 8;
    int wq0 = L_WO + l * 16, wq1 = wq0 + 51200, ofo = OFS > 8 ? WS_FO2 + (mt * 16 + i16) * OFS : WS_FLAGO + mt * 128 + i16 * 8, l16 = l * 16, l8 = l * 8;
    int xma = L_XM + (w * 16 + i16) * 128 + ((g4 ^ (i16 & 7)) * 16), xmb = xma ^ 64, xea = L_XE + (w * 16 + i16) * 32 + g4 * 8;
    int pao = L_PAR + g4 * 80, puo = L_PAR + 1280 + g4 * 48;
    int kdst = l == 0 ? WS_KEY + (cg * 128 + mt) * KS : OOB;
    bdst = WS_BEST + mt * 5120 + cg * 320 + g4 * 16;
    unsigned rowk = 2047 - (mt * 16 + i16);
    const int osrc = opq(WS_O + mt * 20480), ssrc = opq(WS_ST2 + mt * 4096);
    asm volatile("" : "+v"(xro), "+v"(xre), "+v"(wq0), "+v"(wq1), "+v"(ofo), "+v"(l16), "+v"(l8), "+v"(pao), "+v"(puo), "+v"(xma), "+v"(xmb), "+v"(xea));
    asm volatile("" : "+v"(kdst), "+v"(bdst), "+v"(rowk));
    DRAIN();
    ST64(flagv, fo);
    SBAR();
    lds_set(lds, C_AD, w);
    STAMP(8);
    if (ODL) __builtin_amdgcn_s_sleep(ODL);
    wait_flag8<OSL>(rs, ofo, flagv);
    STAMP(9);
    u32x4 of[20];
#pragma unroll
    for (int i = 0; i < O1; ++i) { of[i] = __builtin_amdgcn_raw_buffer_load_b128(rs, l16 + (i & 3) * 1024, osrc + (i >> 2) * 4096, SC1); SBAR(); }
    lds_wait(lds, C_WL, ALL4);
    const u32x4 xa = *(const u32x4*)(lds + xma), xb = *(const u32x4*)(lds + xmb);
    const u32x2 xc = *(const u32x2*)(lds + xea);
    bf16x8 wq[QW];
    auto wfr = [&](int f) __attribute__((always_inline)) { return *(const bf16x8*)(lds + (f < 50 ? wq0 + f * 1024 : wq1 + (f - 50) * 1024)); };
#pragma unroll
    for (int i = 0; i < QW; ++i) wq[i] = wfr(i);
    SBAR();
    f32x4 xf[5];
    xf[0].xy = unpk(xa.x); xf[0].zw = unpk(xa.y); xf[1].xy = unpk(xa.z); xf[1].zw = unpk(xa.w);
    xf[2].xy = unpk(xb.x); xf[2].zw = unpk(xb.y); xf[3].xy = unpk(xb.z); xf[3].zw = unpk(xb.w);
    xf[4].xy = unpk(xc.x); xf[4].zw = unpk(xc.y);
    asm volatile("" : "+v"(xf[0]), "+v"(xf[1]), "+v"(xf[2]), "+v"(xf[3]), "+v"(xf[4]));
    SBAR();
    f32x4 a2[5], ga[5];
    u32x4 ua, ub, uc;
    sfor<0, 100>([&](auto F) {
      constexpr int f = decltype(F)::v, i = f / 5, n = f % 5;
      if constexpr (f == 100 - QW) {
        if (__builtin_expect(rdys != ALL4, 0)) lds_wait(lds, C_PR, ALL4);
        CFENCE();
      }
      if (i == 0) MFMA0(a2[n], wq[f % QW], __builtin_bit_cast(bf16x8, of[i])); else MFMA(a2[n], wq[f % QW], __builtin_bit_cast(bf16x8, of[i]));
      SBAR();
      if constexpr (f + QW < 100) wq[f % QW] = wfr(f + QW);
      if constexpr (f % OG == 0 && O1 + f / OG < 20) {
        constexpr int k = O1 + f / OG;
        of[k] = __builtin_amdgcn_raw_buffer_load_b128(rs, l16 + (k & 3) * 1024, osrc + (k >> 2) * 4096, SC1);
      }
      if constexpr (f == (20 - O1) * OG) lds_set(lds, C_OI, w);
      if constexpr (f == 92 - QW) rdy = lds_cnt(lds, C_PR);
      if constexpr (f == 98 - QW) rdys = __builtin_amdgcn_readfirstlane(rdy);
      constexpr int e = f - (100 - QW);
      if constexpr (e >= 0 && e < 5) ga[e] = *(const f32x4*)(lds + pao + e * 16);
      if constexpr (e == 5) ua = *(const u32x4*)(lds + puo);
      if constexpr (e == 6) ub = *(const u32x4*)(lds + puo + 16);
      if constexpr (e == 7) uc = *(const u32x4*)(lds + puo + 32);
      SBAR();
    });
    STAMP(26);
    float s = 0.f, q = 0.f, d = 0.f;
    {
      const unsigned uh[10] = {ua.x, ua.y, ua.z, ua.w, ub.x, ub.y, ub.z, ub.w, uc.x, uc.y};
#pragma unroll
      for (int n = 0; n < 5; ++n) {
        if (n == 0) asm volatile("s_nop 1" : "+v"(a2[0]));
        if (n == 4) asm volatile("" : "+v"(a2[4]));
        xf[n] = ga[n] * a2[n] + xf[n];
        x1[2 * n] = pack2v(xf[n].xy); x1[2 * n + 1] = pack2v(xf[n].zw);
#pragma unroll
        for (int k = 2 * n; k < 2 * n + 2; ++k) { s = dot2(x1[k], 0x3f803f80u, s); q = dot2(x1[k], x1[k], q); d = dot2(x1[k], uh[k], d); }
        SBAR();
      }
    }
    __builtin_amdgcn_raw_buffer_store_b64(((u32x2){__float_as_uint(rs16(rs32(s, q), rs32(d, __uint_as_float(uc.z)))), tag32}), rs, l8, ssrc + cg * 512, SAUX);
    SBAR();
    STAMP(10);
#pragma unroll
    for (int n = 0; n < 5; ++n) { xf[n].xy = unpk(x1[2 * n]); xf[n].zw = unpk(x1[2 * n + 1]); asm volatile("" : "+v"(xf[n])); }
    f32x4 sb[5], c1[5], gb[5];
#pragma unroll
    for (int n = 0; n < 5; ++n) {
      sb[n] = *(const f32x4*)(lds + pao + 320 + n * 16); c1[n] = *(const f32x4*)(lds + pao + 640 + n * 16); gb[n] = *(const f32x4*)(lds + pao + 960 + n * 16);
    }
#pragma unroll
    for (int n = 0; n < 5; ++n) asm volatile("" : "+v"(sb[n]), "+v"(c1[n]), "+v"(gb[n]));
    auto gather = [&](int so) __attribute__((always_inline)) {
      u32x2 p[8];
      int sp = 0;
      for (;;) {
        asm volatile("" : "+v"(l8));
#pragma unroll
        for (int c = 0; c < 8; ++c) p[c] = __builtin_amdgcn_raw_buffer_load_b64(rs, l8 + c * 512, so, GAUX);
        SBAR();
        lds_set(lds, C_SI, w);
        unsigned bad = 0u;
#pragma unroll
        for (int c = 0; c < 8; c += 2) bad |= off2((u32x4){0u, p[c].y, 0u, p[c + 1].y}, tag32);
        if (none(bad) || ++sp > SPIN_CAP) break;
        __builtin_amdgcn_s_sleep(SRS);
      }
      return add1(add1(add1(__uint_as_float(p[0].x), __uint_as_float(p[1].x)), add1(__uint_as_float(p[2].x), __uint_as_float(p[3].x))),
                  add1(add1(__uint_as_float(p[4].x), __uint_as_float(p[5].x)), add1(__uint_as_float(p[6].x), __uint_as_float(p[7].x))));
    };
    if (SDR) DRAIN();
    if (SDL) __builtin_amdgcn_s_sleep(SDL);
    float U;
    {
      const unsigned v = __float_as_uint(gather(ssrc));
      STAMP(11);
      auto r = __builtin_amdgcn_permlane16_swap(v, v, false, false);
      auto r0 = __builtin_amdgcn_permlane32_swap(r[0], r[0], false, false), r1 = __builtin_amdgcn_permlane32_swap(r[1], r[1], false, false);
      s = __uint_as_float(r0[0]) * (1.0f / DM); q = __uint_as_float(r0[1]); d = __uint_as_float(r1[0]); U = __uint_as_float(r1[1]);
    }
    float var = q * (1.0f / DM) - s * s;
    if (__builtin_expect(__any(s * s > 16.0f * var), 0)) {
      float q2 = 0.f;
#pragma unroll
      for (int k = 0; k < 10; ++k) {
        const float e0 = dot2(x1[k], ONE_LO, -s), e1 = dot2(x1[k], ONE_HI, -s);
        q2 += e0 * e0; q2 += e1 * e1;
      }
      __builtin_amdgcn_raw_buffer_store_b64(((u32x2){__float_as_uint(q2), tag32}), rs, l8, ssrc + 128 * 4096 + cg * 512, SC1);
      var = swapsum32(swapsum16(gather(ssrc + 128 * 4096))) * (1.0f / DM);
    }
    const float mb = rbf(s);
    const float isd = __builtin_amdgcn_rcpf(rbf(__builtin_amdgcn_sqrtf(rbf(rbf(var) + 1e-05f))));
    unsigned key, kmx;
    {
      const float r = (d - mb * U) * isd;
      const unsigned u = __float_as_uint(r);
      unsigned ord = u ^ ((unsigned)((int)u >> 31) | 0x80000000u);
      if (r != r) ord = 0u;
      key = (ord & ~2047u) | rowk;
      kmx = umax(key, dppu<0xB1>(key)); kmx = umax(kmx, dppu<0x4E>(kmx)); kmx = umax(kmx, dppu<0x141>(kmx)); kmx = umax(kmx, dppu<0x140>(kmx));
    }
    ST64(((u32x2){kmx, tag32}), kdst);
    SBAR();
    lds_set(lds, C_KP, w);
    DW_LOAD();
    STAMP(12);
    if (key != kmx) bdst = OOB;
    asm volatile("" : "+v"(bdst));
    const float nmi = -mb * isd;
    unsigned oq[10];
#pragma unroll
    for (int n = 0; n < 5; ++n) {
      const f32x4 h = (xf[n] * isd + nmi) * c1[n] + sb[n];
      __builtin_amdgcn_raw_buffer_store_b128(((u32x4){pack2v(h.xy), tag32, pack2v(h.zw), tag32}), rs, bdst + n * 64, 0, SC1);
      sb[n] = h;
    }
    SBAR();
#pragma unroll
    for (int n = 0; n < 5; ++n) {
      const f32x4 o = gb[n] * sb[n] + xf[n];
      oq[2 * n] = pack2v(o.xy); oq[2 * n + 1] = pack2v(o.zw);
    }
    __builtin_amdgcn_raw_buffer_store_b128(((u32x4){oq[0], oq[1], oq[2], oq[3]}), ro, xro, 0, SC1);
    __builtin_amdgcn_raw_buffer_store_b128(((u32x4){oq[4], oq[5], oq[6], oq[7]}), ro, xro, 64, SC1);
    __builtin_amdgcn_raw_buffer_store_b64(((u32x2){oq[8], oq[9]}), ro, xre, 0, SC1);
    SBAR();
#define X1_PUB(NY) do { \
      vmwait<NY>(); \
      _Pragma("unroll") for (int n = 0; n < 5; ++n) __builtin_amdgcn_raw_buffer_store_b128(((u32x4){x1[2 * n], tag32, x1[2 * n + 1], tag32}), rs, bdst + 2560 + n * 64, 0, SC1); \
      SBAR(); \
    } while (0)
    if (is_gu) MLP_PREP();
    if (!X1L || !is_gu) X1_PUB(0);
    STAMP(20);
    if (!is_gu) { STAMP_DUMP(); return; }
  } else {
    const int j = w - 4;
    int sb[4];
    unsigned ld[4];
#pragma unroll
    for (int i = 0; i < 4; ++i) {
      const int oc = (2 * m0 + 4 * j + i) & 15;
      sb[i] = opq((NOXH && (i & 1)) ? OOB : (rb * 128 + oc * 8) * 1280); ld[i] = opq(lbase + oc * 1024);
    }
    unsigned me = (i16 & 1) ? 0u : 0xffffffffu;
    u32x4 wov[NA], hv[5];
    int hlo, hwa = L_KV + i16 * KVS + g4 * 8, woa = L_WO + j * 25600 + l * 16;
    asm volatile("" : "+v"(me), "+v"(hwa), "+v"(woa));
    const int gq = j * 8 + xcd;
    const char* pfa;
    {
      const int pt = l < 20 ? 0 : l < 40 ? 1 : 2, pr = slot * 20 + min(l - pt * 20, 19);
      pfa = (const char*)(pt == 0 ? wg : pt == 1 ? wu : wd) + (gq < 27 ? (pt < 2 ? pr * FF * 2 + gq * 128 : (gq * 640 + pr) * 128) : 0);
      if (TK < 10) {
        const int r = slot * 2 * TK + min(l - pt * 20, 2 * TK - 1);
        if (pt < 2 || TD != 1) pfa = (const char*)(pt == 0 ? wg : wu) + (gq < 27 ? (r < 32 * TK ? r : r + 320 - 32 * TK) * FF * 2 + gq * 128 : 0);
      }
      asm volatile("" : "+v"(pfa));
    }
    hlo = rb > 0 ? WS_HALO + ((rb - 1) * 16 + hp) * 5120 + l * 16 : OOB;
    const int cc = 32 * j + (l & 31);
    int cbo = WS_CB + ((l >> 5) * 64 + hp) * 2048 + (cc < 120 ? cc : 119) * 16, sgo = WS_STAT + (rb * 128 + cc) * 8;
    int cws = L_CS + (l >> 5) * 512 + cc * 4, sws = L_CS + 1024 + cc * 8, l16 = l * 16;
    asm volatile("" : "+v"(cbo), "+v"(sgo), "+v"(cws), "+v"(sws), "+v"(l16), "+v"(hlo));
    const int wob0 = opq(NOWOL ? OOB : WS_WOT + cg * 102400 + j * 25600);
    int x9a[4];
#pragma unroll
    for (int i = 0; i < 4; ++i) { x9a[i] = L_X + xslot(9) * 16384 + ((2 * m0 + 4 * j + i) & 15) * 1024 + l16; asm volatile("" : "+v"(x9a[i])); }
    int lta = L_LT(j) + g4 * 1024 + (i16 >> 2) * 128 + (i16 & 3) * 8;
    int lsrc = (l >> 3) * 7680 + (l & 7) * 16;
    int ldst = (lu < 800 && !(i16 & 2)) ? WS_MODP + lk * 30720 + ((20 + ll) * 64 + (i16 >> 2) * 16 + g4 * 4 + (i16 & 1) * 2) * 8 : OOB;
    int fpo = WS_FLAGP + (64 * j + l) * 8;
    asm volatile("" : "+v"(lta), "+v"(lsrc), "+v"(ldst), "+v"(fpo));
    const __amdgpu_buffer_rsrc_t rl = __builtin_amdgcn_make_buffer_rsrc((void*)wmod, 0, (lu < 800 && !NOLATE) ? DM * 7680 : 0, 0x00020000);
    const int lso = opq(lk * 32 * 7680 + (20 + ll) * 128);
    MAKE_TAG(5);
    if (TLBP && j == 3) {
      const char* base = (const char*)(l < 5 ? wg : l < 10 ? wu : l < 15 ? wd : l < 25 ? wmod : l < 30 ? (const u16*)out : (const u16*)ws);
      const int i0 = l < 5 ? l : l < 10 ? l - 5 : l < 15 ? l - 10 : l < 25 ? l - 15 : l < 30 ? l - 25 : min(l - 30, 19);
      const char* p = base + i0 * 524288 + b * 128 + ((l >= 30) ? 98304 : 0);
      asm volatile("s_mov_b32 m0, %1\n\ts_nop 0\n\tglobal_load_lds_dword %0, off" :: "v"(p), "s"(lbase + L_DUM) : "m0", "memory");
    }
    asm volatile("" ::"v"(pfc));
#define TOUCH() do { if (!NOTOUCH) asm volatile("s_mov_b32 m0, %1\n\ts_nop 0\n\tglobal_load_lds_dword %0, off" :: "v"(pfa), "s"(lbase + L_DUM) : "m0", "memory"); } while (0)
    u32x4 lb[4];
    {
      READY(cl);
      const u32x4 sc = {silu2(cl.x), silu2(cl.y), silu2(cl.z), silu2(cl.w)};
#pragma unroll
      for (int i = 0; i < 4; ++i) { lb[i] = (i16 >> 2) == i ? sc : (u32x4){0u, 0u, 0u, 0u}; asm volatile("" : "+v"(lb[i])); }
    }
    STAMP(2);
    auto stream = [&](auto F, auto A, auto B) __attribute__((always_inline)) {
      constexpr int f = decltype(F)::v, n = 4 - f;
      sfor<decltype(A)::v, decltype(B)::v>([&](auto I) {
        constexpr int i = decltype(I)::v, pr = i / n, e = f + i % n;
        constexpr int xt = i < XL * n / 4 ? XT1 : XT;
        if constexpr (5 + i >= xt) vmwait<xt - 1>();
        if (i == XT1 - 1) { lds_set(lds, C_XO, j); STAMP(5); }
        if constexpr (XG && i == XL * n / 4) lds_wait(lds, C_HD, 4u);
        static_assert(XT1 <= XT && XT1 <= XL / 2, "");
        if (i == 3 * n + XT - 1) lds_set(lds, C_XA, j);
        if (i == 5 * n + XT - 1) { lds_set(lds, C_XB, j); STAMP(6); }
        dmai<L_X + xslot(pr) * 16384, pr * 128>(rx, xo, sb[e], ld[e]);
      });
      static_assert(XT <= 8, "");
    };
    if (j) stream(IC<0>{}, IC<0>{}, IC<36>{}); else stream(IC<2>{}, IC<0>{}, IC<18>{});
    STAMP(1);
    u32x4 x9[4];
#pragma unroll
    for (int i = 0; i < 4; ++i) LDXI(x9[i], rx, xo, sb[i] + 9 * 128);
    if (j == 0) {
      lds_wait(lds, C_XO, ALL4);
      const int xa = L_X + (2 * m0 + (xcd & 1)) * 1024 + l16;
      u32x4 xr[10];
#pragma unroll
      for (int p = 0; p < 10; ++p) xr[p] = *(const u32x4*)(lds + (p < 9 ? xa + p * 16384 : L_X9 + (xcd & 1) * 1024 + l16));
      float s0 = 0.f, s1 = 0.f, q0 = 0.f, q1 = 0.f;
#pragma unroll
      for (int p = 0; p < 10; ++p) {
        s0 = dot2(xr[p].x, 0x3f803f80u, s0); q0 = dot2(xr[p].x, xr[p].x, q0); s1 = dot2(xr[p].y, 0x3f803f80u, s1); q1 = dot2(xr[p].y, xr[p].y, q1);
        s0 = dot2(xr[p].z, 0x3f803f80u, s0); q0 = dot2(xr[p].z, xr[p].z, q0); s1 = dot2(xr[p].w, 0x3f803f80u, s1); q1 = dot2(xr[p].w, xr[p].w, q1);
      }
      const float mean = sum8(s0 + s1) * (1.0f / DM);
      float var = sum8(q0 + q1) * (1.0f / DM) - mean * mean;
      if (__builtin_expect(__any(mean * mean > 16.0f * var), 0)) {
        float q2 = 0.f;
#pragma unroll
        for (int p = 0; p < 10; ++p)
#pragma unroll
          for (int i = 0; i < 4; ++i) {
            const float d0 = dot2(xr[p][i], ONE_LO, -mean), d1 = dot2(xr[p][i], ONE_HI, -mean);
            q2 += d0 * d0; q2 += d1 * d1;
          }
        var = sum8(q2) * (1.0f / DM);
      }
      ST64(((u32x2){pack2(mean, __builtin_amdgcn_sqrtf(rbf(rbf(var) + 1e-05f))), tag32}), (l & 7) ? OOB : WS_STAT + rc * 64 + l);
    }
    vmwait<4>();
    lds_set(lds, C_XC, j);
    STAMP(4);
    if (HP) __builtin_amdgcn_s_setprio(HP);
    lds_wait(lds, CBG ? C_CH : C_UI, ALL4);
    STAMP(16);
    {
      u32x4 g[4];
      u32x2 sg, fp;
#pragma unroll
      for (int q = 0; q < 4; ++q) LDX(g[q], cbo, q * 32768);
      asm volatile("buffer_load_dwordx2 %0, %1, %2, 0 offen sc1" : "=&v"(sg) : "v"(sgo), "s"(rs) : "memory");
      if (!CBG) lds_wait(lds, C_CH, ALL4);
      asm volatile("buffer_load_dwordx2 %0, %1, %2, 0 offen sc1" : "=&v"(fp) : "v"(fpo), "s"(rs) : "memory");
      vmwait<6>();
#pragma unroll
      for (int i = 0; i < 4; ++i) { READY(x9[i]); *(u32x4*)(lds + x9a[i]) = x9[i]; }
      SBAR();
      vmwait<1>();
      STAMP(19);
      READY(g[0]); READY(g[1]); READY(g[2]); READY(g[3]); READY(sg);
      RETRY4(g, cbo, 32768, 0);
      *(float*)(lds + cws) = rs32(SUM4(g, x), SUM4(g, z));
      if (__builtin_expect(!__all(sg.y == tag32), 0)) {
        int sp = 0;
        do { __builtin_amdgcn_s_sleep(2); sg = ld64(rs, sgo); } while (!__all(sg.y == tag32) && ++sp < SPIN_CAP);
      }
      const f32x2 ms = unpk(sg.x);
      const float r = __builtin_amdgcn_rcpf(ms.y);
      *(f32x2*)(lds + sws) = (f32x2){r, -ms.x * r};
      CFENCE();
      lds_set(lds, C_X9, j);
      STAMP(17);
      vmwait<0>();
      READY(fp);
      if (__builtin_expect(__builtin_amdgcn_ballot_w64(__builtin_bit_cast(u64, fp) != __builtin_bit_cast(u64, flagv)) != 0ull, 0)) wait_flag8(rs, fpo, flagv);
      lds_set(lds, C_FP, j);
      if (HP) __builtin_amdgcn_s_setprio(0);
      STAMP(21);
    }
    lds_wait(lds, C_FP, ALL4);
#define WOLT(a, b) do { _Pragma("unroll") for (int i = (a); i < (b); ++i) { vmwait<TW2 - 1>(); LDXO(wov[i], l16, wob0 + (i >> 2) * 4096, (i & 3) * 1024); } } while (0)
#pragma unroll
    for (int i = 0; i < WP; ++i) { vmwait<TW - 1>(); LDXO(wov[i], l16, wob0 + (i >> 2) * 4096, (i & 3) * 1024); }
    lds_wait(lds, C_KV, ALL4);
    lds_wait(lds, C_HL, ALL4);
    STAMP(27);
    WOLT(WP, WA);
    sfor<0, 4>([&](auto I) { constexpr int i = decltype(I)::v; dmai<i * 1024, 0>(rl, lsrc, lso + i * 8 * 7680, lbase + L_LT(j)); });
    if (PTX == 0) TOUCH();
    WOLT(WA, WB);
    vmwait<WB - WA>();
    STAMP(24);
    if (LPR) __builtin_amdgcn_s_setprio(LPR);
    if (j == 0) {
      lds_wait(lds, C_GO, 1u);
#pragma unroll
      for (int i = 0; i < 5; ++i) LDX(hv[i], hlo, i * 1024);
    }
    {
      f32x4 d = {0.f, 0.f, 0.f, 0.f};
#pragma unroll
      for (int i = 0; i < 4; ++i) {
        const s16x4 lo = trread(lds + lta + i * 32), hi = trread(lds + lta + i * 32 + 512);
        d = __builtin_amdgcn_mfma_f32_16x16x32_bf16(__builtin_shufflevector(lo, hi, 0, 1, 2, 3, 4, 5, 6, 7), __builtin_bit_cast(bf16x8, lb[i]), d, 0, 0, 0);
      }
      ST128(((u32x4){__float_as_uint(me ? d.x : d.z), tag32, __float_as_uint(me ? d.y : d.w), tag32}), ldst);
      SBAR();
    }
    lds_set(lds, C_LD, j);
    STAMP(22);
    if (j == 0) {
      vmwait<1>();
#pragma unroll
      for (int i = 0; i < 5; ++i) READY(hv[i]);
      unsigned bad = 0u;
#pragma unroll
      for (int i = 0; i < 5; ++i) bad |= off2(hv[i], tag32);
      if (__builtin_expect(rb > 0 && !none(bad), 0)) {
        int sp = 0;
        do {
          __builtin_amdgcn_s_sleep(1);
          bad = 0u;
#pragma unroll
          for (int i = 0; i < 5; ++i) hv[i] = ld128(rs, hlo + i * 1024);
#pragma unroll
          for (int i = 0; i < 5; ++i) bad |= off2(hv[i], tag32);
        } while (!none(bad) && ++sp < SPIN_CAP);
      }
#pragma unroll
      for (int i = 0; i < 5; ++i) *(u32x2*)(lds + hwa + i * 32) = (u32x2){hv[i].x, hv[i].z};
      CFENCE();
      lds_set(lds, C_KD + 1, 0);
    }
    if (LPR) __builtin_amdgcn_s_setprio(0);
    STAMP(23);
#pragma unroll
    for (int i = WB; i < NA; ++i) LDXO(wov[i], l16, wob0 + (i >> 2) * 4096, (i & 3) * 1024);
    if (PTX == 2) { lds_wait(lds, C_KD + 1, 1u); TOUCH(); }
    STAMP(18);
    lds_wait(lds, C_LD, ALL4);
    lds_wait(lds, WWG, ALL4);
    vmwait<(PTX == 2 ? 1 : 0)>();
#pragma unroll
    for (int i = 0; i < NA; ++i) READY(wov[i]);
#pragma unroll
    for (int i = 0; i < NA; ++i) *(u32x4*)(lds + woa + i * 1024) = wov[i];
    STAMP(25);
    if (w == RW && b >= 168 && b < 208) {
      int mgo = WS_MODP + (l >> 5) * 30720 + ((b - 148) * 64 + (l & 31) * 2) * 8;
      u32x4 mg[10];
      int sp = 0;
      for (;;) {
        unsigned bad = 0u;
        asm volatile("" : "+v"(mgo));
#pragma unroll
        for (int q = 0; q < 10; ++q) mg[q] = LD128S(mgo, q * 61440);
#pragma unroll
        for (int q = 0; q < 10; ++q) bad |= off2(mg[q], tag32);
        if (none(bad) || ++sp > SPIN_CAP) break;
        __builtin_amdgcn_s_sleep(4);
      }
      f32x2 s = {0.f, 0.f};
#pragma unroll
      for (int q = 0; q < 10; ++q) s += (f32x2){__uint_as_float(mg[q].x), __uint_as_float(mg[q].z)};
      s = rbf2((f32x2){swapsum32(s.x), swapsum32(s.y)});
      if (b >= 188 && b < 198) s = rbf2(s + (f32x2){1.0f, 1.0f});
      ST128(((u32x4){__float_as_uint(s.x), tag32, __float_as_uint(s.y), tag32}), l < 32 ? WS_MODF + ((b - 168) * 64 + l * 2) * 8 : OOB);
    }
    int xeo = (l >> 1) * 1280 + (l & 1) * 16;
    const int xms = (rg * 64 + j * 16) * 1280 + cg * 128;
    asm volatile("" : "+v"(xeo));
    if (XMG == 0) {
      dmai<L_XM, 0>(rx, xo, xms, lbase + j * 2048);
      dmai<L_XM + 1024, 0>(rx, xo, xms + 8 * 1280, lbase + j * 2048);
    }
    lds_wait(lds, C_AD, ALL4);
    if (XMG == 1) {
      dmai<L_XM, 0>(rx, xo, xms, lbase + j * 2048);
      dmai<L_XM + 1024, 0>(rx, xo, xms + 8 * 1280, lbase + j * 2048);
    }
    if (j < 2) dmai<L_XE, 0>(rx, xeo, (rg * 64 + j * 32) * 1280 + 1024 + cg * 32, lbase + j * 1024);
    sfor<NA, 25>([&](auto I) { constexpr int i = decltype(I)::v; dmas<L_WO + i * 1024, (i & 3) * 1024>(rs, l16, wob0 + (i >> 2) * 4096, lbase + j * 25600); });
    DRAIN();
    lds_set(lds, C_WL, j);
    STAMP(8);
    const int pc = l < 32 ? cg * 64 + 2 * l : 512 + cg * 16 + 2 * min(l - 32, 7);
    const int pi = l < 32 ? ((l >> 2) & 3) * 20 + (l >> 4) * 8 + (l & 3) * 2 : (((l - 32) >> 1) & 3) * 20 + 16 + (l & 1) * 2;
    int pgo = WS_MODF + (j * 640 + pc) * 8, pw = l < 40 ? L_PAR + j * 320 + pi * 4 : L_DUM;
    int pwu = l < 40 ? L_PAR + 1280 + (pi / 20) * 48 + (pi % 20) * 2 : L_DUM;
    const __amdgpu_buffer_rsrc_t rwr = __builtin_amdgcn_make_buffer_rsrc((void*)wr, 0, DM * 2, 0x00020000);
    unsigned wrp;
    LDXW(wrp, rwr, l < 40 ? pc * 2 : OOB, 0);
    const __amdgpu_buffer_rsrc_t rgu = __builtin_amdgcn_make_buffer_rsrc((void*)(j < 2 ? wg : wu), 0, DM * FF * 2, 0x00020000);
    int guo = (l >> 1) * (FF * 2) + (l & 1) * 16;
    const int gus = opq((j & 1) * 320 * FF * 2 + fb * 32);
    asm volatile("" : "+v"(pgo), "+v"(pw), "+v"(pwu), "+v"(guo));
#define GU(a, b) do { if (is_gu) sfor<(a), (b)>([&](auto I) { constexpr int i = decltype(I)::v; dmai<L_GU + i * 1024, 0>(rgu, guo, gus + i * 32 * FF * 2, lbase + j * 10240); }); } while (0)
    {
      u32x4 pg;
      if (PW & 4) ST128(((u32x4){0u, ~tag32, 0u, ~tag32}), is_gu ? WS_YP + fb * 5120 + (j + 1) * 1024 + l16 : OOB);
      SBAR();
      if (PDL) __builtin_amdgcn_s_sleep(PDL);
      LDX(pg, pgo, 0);
      GU(0, G1N);
      if (is_gu) vmwait<G1N>(); else vmwait<0>();
      READY(pg); READY(wrp);
      if (__builtin_expect(!none(off2(pg, tag32)), 0)) {
        int sp = 0;
        do { __builtin_amdgcn_s_sleep(4); pg = ld128(rs, pgo); } while (!none(off2(pg, tag32)) && ++sp < SPIN_CAP);
      }
      *(u32x2*)(lds + pw) = (u32x2){pg.x, pg.z};
      if (j == 2) {
        const unsigned uh = pack2v((f32x2){__uint_as_float(pg.x), __uint_as_float(pg.z)} * unpk(wrp));
        *(unsigned*)(lds + pwu) = uh;
        const float us = sum64(dot2(uh, 0x3f803f80u, 0.f)) * 0.25f;
        *(float*)(lds + L_PAR + 1280 + g4 * 48 + 40) = us;
      }
      CFENCE();
      lds_set(lds, C_PR, j);
    }
    STAMP(9);
    if (TK < 10 && slot >= 16 && j * 8 + xcd < 27) {
      constexpr int NR = 4 * (10 - TK);
      const int r = (slot - 16) * NR + min(l & 31, NR - 1);
      const char* p0 = (const char*)(l < 32 ? wg : wu) + (r < 8 * NR ? r + 32 * TK : r + 64 * TK) * FF * 2 + gq * 128;
      const char* p1 = (const char*)wd + (gq * 640 + (slot - 16) * 40 + min(l, 39)) * 128;
      asm volatile("" : "+v"(p0), "+v"(p1));
      lds_wait(lds, T2G, ALL4);
      asm volatile("s_mov_b32 m0, %1\n\ts_nop 0\n\tglobal_load_lds_dword %0, off" :: "v"(p0), "s"(lbase + L_DUM) : "m0", "memory");
      if (TD == 0) asm volatile("global_load_lds_dword %0, off" :: "v"(p1) : "memory");
      static_assert(NR <= 32, "");
    }
    if (is_gu) {
      if (G2N > G1N) lds_wait(lds, C_OI, ALL4);
      GU(G1N, G2N);
      lds_wait(lds, C_SI, ALL4);
      if (DWF) DW_LOAD();
      GU(G2N, 10);
      if (!DWF) DW_LOAD();
      vmwait<(DWF ? 0 : 4)>();
      lds_set(lds, C_GL, j);
      MLP_PREP();
      STAMP(20);
    } else {
      if (!is_dn) { DRAIN(); STAMP_DUMP(); return; }
      const int col = (dnb >> 3) * 128 + (dnb & 1) * 64 + (l & 7) * 8 + ((dnb >> 1) & 3) * 2 + (l >> 5);
      float V, gbv;
      lds_wait(lds, C_SI, ALL4);
      {
        u32x4 g[5];
        u32x2 gg;
        unsigned wv[5];
        int sp = 0;
#pragma unroll
        for (int i = 0; i < 5; ++i) wv[i] = __builtin_amdgcn_raw_buffer_load_b32(rwr, l * 4, i * 256, 0);
        for (;;) {
          unsigned bad = 0u;
#pragma unroll
          for (int i = 0; i < 5; ++i) g[i] = ld128(rs, WS_MODF + 5120 + i * 1024 + l * 16);
          gg = ld64(rs, WS_MODF + (1920 + col) * 8);
#pragma unroll
          for (int i = 0; i < 5; ++i) bad |= off2(g[i], tag32);
          if (none(bad | (gg.y ^ tag32)) || ++sp > SPIN_CAP) break;
          __builtin_amdgcn_s_sleep(4);
        }
        V = 0.f;
#pragma unroll
        for (int i = 0; i < 5; ++i) { const f32x2 f = unpk(wv[i]); V += __uint_as_float(g[i].x) * f.x; V += __uint_as_float(g[i].z) * f.y; }
        V = sum64(V);
        gbv = __uint_as_float(gg.x);
      }
      if (j == 0) {
        u32x4 kk;
        int sp = 0;
        lds_wait(lds, C_KP, ALL4);
        __builtin_amdgcn_s_sleep(KN);
        for (;;) {
          kk = keys(rs, xcd, l);
          if (none(off2(kk, tag32)) || ++sp > SPIN_CAP) break;
          __builtin_amdgcn_s_sleep(6);
        }
        const unsigned kmax = __builtin_amdgcn_readfirstlane(max64(umax(kk.x, kk.z)));
        if (l == 0) *(unsigned*)(lds + L_MISC + 192) = kmax;
        CFENCE();
        lds_set(lds, C_SL, 0);
      } else lds_wait(lds, C_SL, 1u);
      STAMP(13);
      const unsigned kmax = *(const unsigned*)(lds + L_MISC + 192);
      const unsigned ord = (kmax & ~2047u) | 1024u;
      const float rsel = rbf(__uint_as_float((ord & 0x80000000u) ? (ord & 0x7fffffffu) : ~ord) + V);
      int bo = WS_BEST + ((2047 - (int)(kmax & 2047u)) >> 4) * 5120 + ((col >> 1) & 1) * 8 +
               (col < 512 ? (col >> 6) * 320 + (((col >> 5) & 1) * 2 + ((col >> 2) & 1)) * 64 + ((col >> 3) & 3) * 16
                          : ((col - 512) >> 4) * 320 + 256 + ((col >> 2) & 3) * 16);
      unsigned hs = l < 32 ? 16u : 0u;
      float sg = rbf(__builtin_amdgcn_rcpf(1.0f + __expf(-rsel)));
      int odo = (l & 24) == 0 ? ((2047 - (int)(kmax & 2047u)) * DM + col) * 2 : OOB;
      int yo = WS_YP + (27 * j + (l >> 3)) * 5120 + dnb * 128 + (l & 7) * 16, yo3 = yo + (l < 24 ? 24 * 5120 : 0);
      unsigned cnt = l == 0 ? 1u : 0u, ca = lbase + L_CNT + ((C_YD + l) & 63) * 4;
      int yw = L_MRED + l * 4;
      asm volatile("" : "+v"(bo), "+v"(hs), "+v"(gbv), "+v"(sg), "+v"(odo), "+v"(cnt), "+v"(ca), "+v"(yw));
      u32x4 yv[4];
      u32x2 hv2, xv2;
      int spins = 0;
      __builtin_amdgcn_s_sleep(YD);
      for (;;) {
        asm volatile("" : "+v"(yo), "+v"(yo3), "+v"(bo));
        hv2 = __builtin_amdgcn_raw_buffer_load_b64(rs, bo, 0, SC1); xv2 = __builtin_amdgcn_raw_buffer_load_b64(rs, bo + 2560, 0, SC1);
        yv[0] = LD128S(yo, 0); yv[1] = LD128S(yo, 8 * 5120); yv[2] = LD128S(yo, 16 * 5120); yv[3] = LD128S(yo3, 0);
        if (none(off2((u32x4){0u, hv2.y, 0u, xv2.y}, tag32) | off2(yv[0], tag32) | off2(yv[1], tag32) | off2(yv[2], tag32) | off2(yv[3], tag32)) || ++spins > SPIN_CAP) break;
        __builtin_amdgcn_s_sleep(YS);
      }
      STAMP(14);
      if (l >= 24) { yv[3].x = 0u; yv[3].z = 0u; }
      *(float*)(lds + yw + j * 256) = rs32(SUM4(yv, x), SUM4(yv, z));
      asm volatile("ds_add_rtn_u32 %0, %1, %0\n\ts_waitcnt lgkmcnt(0)" : "+v"(cnt) : "v"(ca) : "memory");
      if (__builtin_amdgcn_readfirstlane(cnt) == 3u) {
        const float* src = (const float*)(lds + yw);
        float v = swapsum16((src[0] + src[64]) + (src[128] + src[192]));
        v += dppf<0x128>(v);
        const float h2 = __uint_as_float((hv2.x << hs) & 0xffff0000u), x1v = __uint_as_float((xv2.x << hs) & 0xffff0000u);
        const float y = rbf(rbf(v) * sg);
        const float hh = rbf(h2 + y);
        __builtin_amdgcn_raw_buffer_store_b16((u16)(pack2(x1v + rbf(gbv * hh), 0.f) & 0xffffu), ro, odo, 0, SC1);
      }
      STAMP(15);
      STAMP_DUMP();
      return;
    }
  }

  {
    float z0 = 0.f, z1 = 0.f;
    STAMP(19);
    if (w == 7) {
      u32x4 kk;
      int sp = 0;
      int r0 = l * 16, r2 = 2048 + (l & 31) * 16, hw[3];
#pragma unroll
      for (int i = 0; i < 3; ++i) {
        const int e = i < 2 ? i * 64 + l : 128 + (l & 31), c = (e * 205) >> 12, n = (e - c * 20) >> 2, g = e & 3;
        hw[i] = L_H2 + (n < 4 ? c * 128 + (n >> 1) * 64 + g * 16 + (n & 1) * 8 : 1024 + c * 32 + g * 8);
        asm volatile("" : "+v"(hw[i]));
      }
      lds_wait(lds, C_KP, ALL4);
      if (KG) __builtin_amdgcn_s_sleep(KG);
      for (;;) {
        kk = keys(rs, xcd, l);
        if (none(off2(kk, tag32)) || ++sp > SPIN_CAP) break;
        __builtin_amdgcn_s_sleep(3);
      }
      const int sel = 2047 - (int)(__builtin_amdgcn_readfirstlane(max64(umax(kk.x, kk.z))) & 2047u);
      const int so = WS_BEST + (sel >> 4) * 5120;
      u32x4 r[3];
      sp = 0;
      for (;;) {
        asm volatile("" : "+v"(r0), "+v"(r2));
        r[0] = LD128S(r0, so); r[1] = LD128S(r0 + 1024, so); r[2] = LD128S(r2, so);
        if (none(off2(r[0], tag32) | off2(r[1], tag32) | off2(r[2], tag32)) || ++sp > SPIN_CAP) break;
        __builtin_amdgcn_s_sleep(1);
      }
#pragma unroll
      for (int i = 0; i < 3; ++i) *(u32x2*)(lds + hw[i]) = (u32x2){r[i].x, r[i].z};
    }
    LBAR();
    STAMP(13);
    {
      f32x4 ga = {0.f, 0.f, 0.f, 0.f};
#pragma unroll
      for (int i = 0; i < 5; ++i) ga = __builtin_amdgcn_mfma_f32_16x16x32_bf16(af[i], *(const bf16x8*)(lds + hbo + i * 256), ga, 0, 0, 0);
      *(f32x4*)(lds + mro) = ga;
      LBAR();
      if (w >= 3) {
        const float* src = (const float*)(lds + L_MRED) + g4 * 4 + (l & 3);
        const float gg = rbf((src[0] + src[16]) + (src[32] + src[48]));
        const float uu = rbf((src[64] + src[80]) + (src[96] + src[112]));
        const float sg = rbf(gg * __builtin_amdgcn_rcpf(1.0f + __expf(-gg)));
        const float act = sg * uu;
        const unsigned ap = pack2(act, dppf<0xB1>(act));
        const unsigned a01 = dppu<0x00>(ap), a23 = dppu<0xAA>(ap);
        float y[8];
#pragma unroll
        for (int j = 0; j < 8; ++j) y[j] = dot2(dp[1][j], a23, dot2(dp[0][j], a01, 0.f));
        const float u0 = rs32(y[0], y[4]), u1 = rs32(y[1], y[5]), u2 = rs32(y[2], y[6]), u3 = rs32(y[3], y[7]);
        z0 = rs16(u0, u2); z1 = rs16(u1, u3);
      }
    }
    ST128(((u32x4){__float_as_uint(z0), tag32, __float_as_uint(z1), tag32}), ydst);
    SBAR();
    STAMP(14);
    if (X1L && w < 4) X1_PUB(1);
    if (b == 0 && t == 0) {
      const u64 ne = (((u64)ep1 << 32) | ep0) + 1ull;
      ST64(((u32x2){(unsigned)ne, (unsigned)(ne >> 32)}), WS_EPOCH);
    }
    STAMP_DUMP();
  }
}

extern "C" void kernel_launch(void* const* d_in, const int* in_sizes, int n_in, void* d_out, int out_size,
                              void* d_ws, size_t ws_size, hipStream_t stream) {
  k_all<<<256, 512, 0, stream>>>((const u16*)d_in[0], (const u16*)d_in[1], (const u16*)d_in[2], (const u16*)d_in[4],
                                 (const u16*)d_in[5], (const u16*)d_in[6], (const u16*)d_in[7], (const u16*)d_in[8],
                                 (const u16*)d_in[9], (char*)d_ws, (u16*)d_out);
}
```
